```python
import torch

def _softmax(x, axis=-1):
    return torch.softmax(x, dim=axis)
def _gelu(x):
    return torch.nn.functional.gelu(x, approximate="tanh")
def _silu(x):
    return torch.nn.functional.silu(x)
def _sigmoid(x):
    return torch.sigmoid(x)
def _layernorm(x, axis=-1, eps=1e-5):
    m = x.mean(dim=axis, keepdim=True)
    v = x.var(dim=axis, keepdim=True, unbiased=False)
    return (x - m) / torch.sqrt(v + eps)
def _scatter_add(tgt, idx, src, axis=0):
    return tgt.clone().index_add_(axis, idx, src)
def _segment_sum(src, seg, n):
    out = torch.zeros((n,) + src.shape[1:], dtype=src.dtype)
    return out.index_add_(0, seg, src)
def _pad(x, pw):
    flat = tuple(v for pair in reversed(pw) for v in pair)
    return torch.nn.functional.pad(x, flat)

kernel_name = 'concat_heads__sliding_local_dilated__mha_block__prompt'

def setup_inputs():

    _w = (lambda a: (lambda t: t.to(torch.bfloat16) if t.is_floating_point() else t)(torch.as_tensor(a)))
    def _A_setup():
        B, S, D = 1, 2048, 448
        return {
            "x": _w((lambda *s: torch.randn(*s, dtype=torch.bfloat16) * 0.4)(B, S, D)),
            "wqkv": _w((lambda *s: torch.randn(*s, dtype=torch.bfloat16) * 0.8)(D, 3 * D)),
            "wo": _w((lambda *s: torch.randn(*s, dtype=torch.bfloat16) * 0.1)(D, D)),
        }
    _A_in = _A_setup()

    def _B_setup():
        B, S, D, H = 1, 2048, 448, 16
        return {
            "x": _w((lambda *s: torch.randn(*s, dtype=torch.bfloat16) * 0.2)(B, S, D)),
            "wqkv": _w((lambda *s: torch.randn(*s, dtype=torch.bfloat16) * 0.8)(D, 3 * D)),
            "wo": _w((lambda *s: torch.randn(*s, dtype=torch.bfloat16) * 0.2)(D, D)),
        }
    _B_in = _B_setup()

    return {
        "A_x": _A_in["x"],
        "A_wqkv": _A_in["wqkv"],
        "A_wo": _A_in["wo"],
        "B_x": _B_in["x"],
        "B_wqkv": _B_in["wqkv"],
        "B_wo": _B_in["wo"],
    }

def reference(A_x, A_wqkv, A_wo, B_x, B_wqkv, B_wo):
    def _A_ref(x, wqkv, wo):
        B, S, D = x.shape
        H = 16; Dh = D // H
        W, dil = 8, 2
        qkv = (lambda x, *d: x.permute(*d))((x @ wqkv).reshape(B, S, 3, H, Dh), 2, 0, 3, 1, 4)
        q, k, v = qkv[0], qkv[1], qkv[2]
        scale = Dh ** -0.5
        pos = torch.arange(S)
        off = torch.arange(W) * dil
        idx = pos[:, None] - off[None, :]
        mask = idx >= 0
        idx_c = torch.where(mask, idx, 0).reshape(-1)
        kg = k[:, :, idx_c, :].reshape(B, H, S, W, Dh)
        vg = v[:, :, idx_c, :].reshape(B, H, S, W, Dh)
        logits = torch.einsum("bhsd,bhswd->bhsw", q, kg) * scale
        logits = torch.where(mask[None, None, :, :], logits, -1e9)
        attn = _softmax(logits, axis=-1)
        o = torch.einsum("bhsw,bhswd->bhsd", attn, vg)
        return (lambda x, *d: x.permute(*d))(o, 0, 2, 1, 3).reshape(B, S, D) @ wo

    def _B_ref(x, wqkv, wo):
        B, S, D = x.shape
        H = 16
        Dh = D // H
        qkv = (lambda x, *d: x.permute(*d))((x @ wqkv).reshape(B, S, 3, H, Dh), 2, 0, 3, 1, 4)
        q, k, v = qkv[0], qkv[1], qkv[2]
        scores = torch.einsum("bhqd,bhkd->bhqk", q, k) * (Dh ** -0.5)
        p = _softmax(scores, axis=-1)
        o = torch.einsum("bhqk,bhkd->bhqd", p, v)
        return (lambda x, *d: x.permute(*d))(o, 0, 2, 1, 3).reshape(B, S, D) @ wo

    a = _A_ref(A_x, A_wqkv, A_wo)
    b = _B_ref(B_x, B_wqkv, B_wo)
    return torch.concatenate([a, b], axis=-1)
```

```cpp
#include <hip/hip_runtime.h>
#include <hip/hip_bf16.h>
#include <stdint.h>

typedef unsigned short u16;
__device__ __forceinline__ float bf2f(u16 v) { return __uint_as_float(((unsigned)v) << 16); }
__device__ __forceinline__ u16 f2bf(float f) { unsigned u = __float_as_uint(f); u += 0x7fff + ((u >> 16) & 1); return (u16)(u >> 16); }
__device__ __forceinline__ float rbf(float f) { return bf2f(f2bf(f)); }

#define MIMIC 1

constexpr int S = 2048, D = 448, H = 16, DH = 28;

__global__ void gemm_naive(const u16* A, const u16* B, u16* C, int M, int N, int K, int ldc) {
    int n = blockIdx.x * blockDim.x + threadIdx.x;
    int m = blockIdx.y;
    if (n >= N) return;
    float acc = 0.f;
    for (int k = 0; k < K; ++k) acc += bf2f(A[m * K + k]) * bf2f(B[k * N + n]);
    C[m * ldc + n] = f2bf(acc);
}

__global__ void attnA_naive(const u16* qkv, u16* o) {
    int idx = blockIdx.x * blockDim.x + threadIdx.x;
    int s = idx / H, h = idx % H;
    const float scale = MIMIC ? 0.1889822365046136f : 0.1889822365046136f;
    float lg[8]; float mx = -3e38f;
    #pragma unroll
    for (int w = 0; w < 8; ++w) {
        int j = s - 2 * w;
        float a = 0.f;
        if (j >= 0) {
            for (int d = 0; d < DH; ++d) a += bf2f(qkv[s * 3 * D + h * DH + d]) * bf2f(qkv[j * 3 * D + D + h * DH + d]);
            if (MIMIC) a = rbf(rbf(a) * scale); else a *= scale;
        } else a = -1e9f;
        lg[w] = a; mx = fmaxf(mx, a);
    }
    float l = 0.f;
    #pragma unroll
    for (int w = 0; w < 8; ++w) { lg[w] = __expf(lg[w] - mx); l += lg[w]; }
    float rl = 1.f / l;
    #pragma unroll
    for (int w = 0; w < 8; ++w) { lg[w] *= rl; if (MIMIC) lg[w] = rbf(lg[w]); }
    for (int d = 0; d < DH; ++d) {
        float a = 0.f;
        #pragma unroll
        for (int w = 0; w < 8; ++w) { int j = s - 2 * w; if (j < 0) j = 0; a += lg[w] * bf2f(qkv[j * 3 * D + 2 * D + h * DH + d]); }
        o[s * D + h * DH + d] = f2bf(a);
    }
}

__global__ void attnB_naive(const u16* qkv, u16* o) {
    __shared__ float p[S];
    __shared__ float red[256];
    __shared__ float q[DH];
    int s = blockIdx.x / H, h = blockIdx.x % H, t = threadIdx.x;
    const float scale = 0.1889822365046136f;
    if (t < DH) q[t] = bf2f(qkv[s * 3 * D + h * DH + t]);
    __syncthreads();
    float mx = -3e38f;
    for (int j = t; j < S; j += 256) {
        float a = 0.f;
        for (int d = 0; d < DH; ++d) a += q[d] * bf2f(qkv[j * 3 * D + D + h * DH + d]);
        if (MIMIC) a = rbf(rbf(a) * scale); else a *= scale;
        p[j] = a; mx = fmaxf(mx, a);
    }
    red[t] = mx; __syncthreads();
    for (int o2 = 128; o2 > 0; o2 >>= 1) { if (t < o2) red[t] = fmaxf(red[t], red[t + o2]); __syncthreads(); }
    mx = red[0]; __syncthreads();
    float l = 0.f;
    for (int j = t; j < S; j += 256) { float e = __expf(p[j] - mx); p[j] = e; l += e; }
    red[t] = l; __syncthreads();
    for (int o2 = 128; o2 > 0; o2 >>= 1) { if (t < o2) red[t] += red[t + o2]; __syncthreads(); }
    l = red[0]; __syncthreads();
    float rl = 1.f / l;
    int g = t >> 5, d = t & 31;
    float a = 0.f;
    if (d < DH) for (int j = g * 256; j < g * 256 + 256; ++j) { float pp = p[j] * rl; if (MIMIC) pp = rbf(pp); a += pp * bf2f(qkv[j * 3 * D + 2 * D + h * DH + d]); }
    red[t] = a; __syncthreads();
    if (t < DH) { float r = 0.f; for (int gg = 0; gg < 8; ++gg) r += red[gg * 32 + t]; o[s * D + h * DH + t] = f2bf(r); }
}

extern "C" void kernel_launch(void* const* d_in, const int* in_sizes, int n_in,
                              void* d_out, int out_size, void* d_ws, size_t ws_size,
                              hipStream_t stream) {
    const u16* xA = (const u16*)d_in[0]; const u16* wqkvA = (const u16*)d_in[1]; const u16* woA = (const u16*)d_in[2];
    const u16* xB = (const u16*)d_in[3]; const u16* wqkvB = (const u16*)d_in[4]; const u16* woB = (const u16*)d_in[5];
    u16* out = (u16*)d_out;
    u16* qkvA = (u16*)d_ws; u16* qkvB = qkvA + S * 3 * D; u16* oA = qkvB + S * 3 * D; u16* oB = oA + S * D;
    gemm_naive<<<dim3((3 * D + 255) / 256, S), 256, 0, stream>>>(xA, wqkvA, qkvA, S, 3 * D, D, 3 * D);
    gemm_naive<<<dim3((3 * D + 255) / 256, S), 256, 0, stream>>>(xB, wqkvB, qkvB, S, 3 * D, D, 3 * D);
    attnA_naive<<<S * H / 256, 256, 0, stream>>>(qkvA, oA);
    attnB_naive<<<S * H, 256, 0, stream>>>(qkvB, oB);
    gemm_naive<<<dim3((D + 255) / 256, S), 256, 0, stream>>>(oA, woA, out, S, D, D, 2 * D);
    gemm_naive<<<dim3((D + 255) / 256, S), 256, 0, stream>>>(oB, woB, out + D, S, D, D, 2 * D);
}
```

```cpp
#include <hip/hip_runtime.h>
#include <hip/hip_bf16.h>
#include <stdint.h>

typedef unsigned short u16;
__device__ __forceinline__ float bf2f(u16 v) { return __uint_as_float(((unsigned)v) << 16); }
__device__ __forceinline__ u16 f2bf(float f) { unsigned u = __float_as_uint(f); u += 0x7fff + ((u >> 16) & 1); return (u16)(u >> 16); }
__device__ __forceinline__ float rbf(float f) { return bf2f(f2bf(f)); }

#define MIMIC 0

constexpr int S = 2048, D = 448, H = 16, DH = 28;

__global__ void gemm_naive(const u16* A, const u16* B, u16* C, int M, int N, int K, int ldc) {
    int n = blockIdx.x * blockDim.x + threadIdx.x;
    int m = blockIdx.y;
    if (n >= N) return;
    float acc = 0.f;
    for (int k = 0; k < K; ++k) acc += bf2f(A[m * K + k]) * bf2f(B[k * N + n]);
    C[m * ldc + n] = f2bf(acc);
}

__global__ void attnA_naive(const u16* qkv, u16* o) {
    int idx = blockIdx.x * blockDim.x + threadIdx.x;
    int s = idx / H, h = idx % H;
    const float scale = MIMIC ? 0.1889822365046136f : 0.1889822365046136f;
    float lg[8]; float mx = -3e38f;
    #pragma unroll
    for (int w = 0; w < 8; ++w) {
        int j = s - 2 * w;
        float a = 0.f;
        if (j >= 0) {
            for (int d = 0; d < DH; ++d) a += bf2f(qkv[s * 3 * D + h * DH + d]) * bf2f(qkv[j * 3 * D + D + h * DH + d]);
            if (MIMIC) a = rbf(rbf(a) * scale); else a *= scale;
        } else a = -1e9f;
        lg[w] = a; mx = fmaxf(mx, a);
    }
    float l = 0.f;
    #pragma unroll
    for (int w = 0; w < 8; ++w) { lg[w] = __expf(lg[w] - mx); l += lg[w]; }
    float rl = 1.f / l;
    #pragma unroll
    for (int w = 0; w < 8; ++w) { lg[w] *= rl; if (MIMIC) lg[w] = rbf(lg[w]); }
    for (int d = 0; d < DH; ++d) {
        float a = 0.f;
        #pragma unroll
        for (int w = 0; w < 8; ++w) { int j = s - 2 * w; if (j < 0) j = 0; a += lg[w] * bf2f(qkv[j * 3 * D + 2 * D + h * DH + d]); }
        o[s * D + h * DH + d] = f2bf(a);
    }
}

__global__ void attnB_naive(const u16* qkv, u16* o) {
    __shared__ float p[S];
    __shared__ float red[256];
    __shared__ float q[DH];
    int s = blockIdx.x / H, h = blockIdx.x % H, t = threadIdx.x;
    const float scale = 0.1889822365046136f;
    if (t < DH) q[t] = bf2f(qkv[s * 3 * D + h * DH + t]);
    __syncthreads();
    float mx = -3e38f;
    for (int j = t; j < S; j += 256) {
        float a = 0.f;
        for (int d = 0; d < DH; ++d) a += q[d] * bf2f(qkv[j * 3 * D + D + h * DH + d]);
        if (MIMIC) a = rbf(rbf(a) * scale); else a *= scale;
        p[j] = a; mx = fmaxf(mx, a);
    }
    red[t] = mx; __syncthreads();
    for (int o2 = 128; o2 > 0; o2 >>= 1) { if (t < o2) red[t] = fmaxf(red[t], red[t + o2]); __syncthreads(); }
    mx = red[0]; __syncthreads();
    float l = 0.f;
    for (int j = t; j < S; j += 256) { float e = __expf(p[j] - mx); p[j] = e; l += e; }
    red[t] = l; __syncthreads();
    for (int o2 = 128; o2 > 0; o2 >>= 1) { if (t < o2) red[t] += red[t + o2]; __syncthreads(); }
    l = red[0]; __syncthreads();
    float rl = 1.f / l;
    int g = t >> 5, d = t & 31;
    float a = 0.f;
    if (d < DH) for (int j = g * 256; j < g * 256 + 256; ++j) { float pp = p[j] * rl; if (MIMIC) pp = rbf(pp); a += pp * bf2f(qkv[j * 3 * D + 2 * D + h * DH + d]); }
    red[t] = a; __syncthreads();
    if (t < DH) { float r = 0.f; for (int gg = 0; gg < 8; ++gg) r += red[gg * 32 + t]; o[s * D + h * DH + t] = f2bf(r); }
}

#define AMP 200
__device__ __forceinline__ void spin_amp(unsigned long long r0, unsigned long long r1) {
    unsigned long long tgt = r1 + (r1 - r0) * AMP;
    while (__builtin_amdgcn_s_memrealtime() < tgt) __builtin_amdgcn_s_sleep(8);
}
__global__ void probe_lat(const unsigned* p, int strideDw, unsigned* sink) {
    unsigned long long r0 = __builtin_amdgcn_s_memrealtime();
    unsigned off = 0;
    for (int i = 0; i < 16; ++i) { unsigned v; const unsigned* a = p + off + i * strideDw; asm volatile("global_load_dword %0, %1, off\n\ts_waitcnt vmcnt(0)" : "=v"(v) : "v"(a) : "memory"); off = (v & 0) ; asm volatile("" : "+v"(off)); }
    unsigned long long r1 = __builtin_amdgcn_s_memrealtime();
    spin_amp(r0, r1);
    if (off == 12345) sink[0] = off;
}
template<int NP>
__global__ void __launch_bounds__(512) probe_bw(const char* x, int share, int xcdstride, int nreg, unsigned* sink) {
    extern __shared__ __attribute__((aligned(16))) char sm[];
    unsigned long long r0 = __builtin_amdgcn_s_memrealtime();
    const int lane = threadIdx.x & 63, w = __builtin_amdgcn_readfirstlane(threadIdx.x >> 6);
    const int xcd = blockIdx.x & 7, j = blockIdx.x >> 3;
    const int region = (xcd * xcdstride + (j % share)) % nreg;
    const char* base = x + (size_t)region * NP * 8 * 1024;
    #pragma unroll
    for (int i = 0; i < NP; ++i) {
        int piece = i * 8 + w;
        const char* a = base + piece * 1024 + lane * 16;
        __builtin_amdgcn_global_load_lds((const __attribute__((address_space(1))) void*)a, (__attribute__((address_space(3))) void*)(sm + piece * 1024), 16, 0, 0);
    }
    asm volatile("s_waitcnt vmcnt(0)" ::: "memory");
    __syncthreads();
    unsigned long long r1 = __builtin_amdgcn_s_memrealtime();
    spin_amp(r0, r1);
    if (((unsigned*)sm)[threadIdx.x] == 0x12345678 && share < 0) sink[0] = 1;
}
__global__ void probe_icache(unsigned* sink) {
    unsigned long long r0 = __builtin_amdgcn_s_memrealtime();
    float a = threadIdx.x;
    asm volatile(".rept 4096\n\tv_add_f32_e64 %0, %0, %0\n\t.endr" : "+v"(a));
    unsigned long long r1 = __builtin_amdgcn_s_memrealtime();
    spin_amp(r0, r1);
    if (a == 1.2345f) sink[0] = 1;
}

extern "C" void kernel_launch(void* const* d_in, const int* in_sizes, int n_in,
                              void* d_out, int out_size, void* d_ws, size_t ws_size,
                              hipStream_t stream) {
    const u16* xA = (const u16*)d_in[0]; const u16* wqkvA = (const u16*)d_in[1]; const u16* woA = (const u16*)d_in[2];
    const u16* xB = (const u16*)d_in[3]; const u16* wqkvB = (const u16*)d_in[4]; const u16* woB = (const u16*)d_in[5];
    u16* out = (u16*)d_out;
    u16* qkvA = (u16*)d_ws; u16* qkvB = qkvA + S * 3 * D; u16* oA = qkvB + S * 3 * D; u16* oB = oA + S * D;
    hipFuncSetAttribute((const void*)probe_bw<14>, hipFuncAttributeMaxDynamicSharedMemorySize, 120*1024); probe_bw<14><<<256, 512, 120*1024, stream>>>((const char*)xB, 4, 4, 16, (unsigned*)d_ws);
    gemm_naive<<<dim3((3 * D + 255) / 256, S), 256, 0, stream>>>(xA, wqkvA, qkvA, S, 3 * D, D, 3 * D);
    gemm_naive<<<dim3((3 * D + 255) / 256, S), 256, 0, stream>>>(xB, wqkvB, qkvB, S, 3 * D, D, 3 * D);
    attnA_naive<<<S * H / 256, 256, 0, stream>>>(qkvA, oA);
    attnB_naive<<<S * H, 256, 0, stream>>>(qkvB, oB);
    gemm_naive<<<dim3((D + 255) / 256, S), 256, 0, stream>>>(oA, woA, out, S, D, D, 2 * D);
    gemm_naive<<<dim3((D + 255) / 256, S), 256, 0, stream>>>(oB, woB, out + D, S, D, D, 2 * D);
}
```

```cpp
#include <hip/hip_runtime.h>
#include <hip/hip_bf16.h>
#include <stdint.h>

typedef unsigned short u16;
__device__ __forceinline__ float bf2f(u16 v) { return __uint_as_float(((unsigned)v) << 16); }
__device__ __forceinline__ u16 f2bf(float f) { unsigned u = __float_as_uint(f); u += 0x7fff + ((u >> 16) & 1); return (u16)(u >> 16); }
__device__ __forceinline__ float rbf(float f) { return bf2f(f2bf(f)); }

#define MIMIC 0

constexpr int S = 2048, D = 448, H = 16, DH = 28;

__global__ void gemm_naive(const u16* A, const u16* B, u16* C, int M, int N, int K, int ldc) {
    int n = blockIdx.x * blockDim.x + threadIdx.x;
    int m = blockIdx.y;
    if (n >= N) return;
    float acc = 0.f;
    for (int k = 0; k < K; ++k) acc += bf2f(A[m * K + k]) * bf2f(B[k * N + n]);
    C[m * ldc + n] = f2bf(acc);
}

__global__ void attnA_naive(const u16* qkv, u16* o) {
    int idx = blockIdx.x * blockDim.x + threadIdx.x;
    int s = idx / H, h = idx % H;
    const float scale = MIMIC ? 0.1889822365046136f : 0.1889822365046136f;
    float lg[8]; float mx = -3e38f;
    #pragma unroll
    for (int w = 0; w < 8; ++w) {
        int j = s - 2 * w;
        float a = 0.f;
        if (j >= 0) {
            for (int d = 0; d < DH; ++d) a += bf2f(qkv[s * 3 * D + h * DH + d]) * bf2f(qkv[j * 3 * D + D + h * DH + d]);
            if (MIMIC) a = rbf(rbf(a) * scale); else a *= scale;
        } else a = -1e9f;
        lg[w] = a; mx = fmaxf(mx, a);
    }
    float l = 0.f;
    #pragma unroll
    for (int w = 0; w < 8; ++w) { lg[w] = __expf(lg[w] - mx); l += lg[w]; }
    float rl = 1.f / l;
    #pragma unroll
    for (int w = 0; w < 8; ++w) { lg[w] *= rl; if (MIMIC) lg[w] = rbf(lg[w]); }
    for (int d = 0; d < DH; ++d) {
        float a = 0.f;
        #pragma unroll
        for (int w = 0; w < 8; ++w) { int j = s - 2 * w; if (j < 0) j = 0; a += lg[w] * bf2f(qkv[j * 3 * D + 2 * D + h * DH + d]); }
        o[s * D + h * DH + d] = f2bf(a);
    }
}

__global__ void attnB_naive(const u16* qkv, u16* o) {
    __shared__ float p[S];
    __shared__ float red[256];
    __shared__ float q[DH];
    int s = blockIdx.x / H, h = blockIdx.x % H, t = threadIdx.x;
    const float scale = 0.1889822365046136f;
    if (t < DH) q[t] = bf2f(qkv[s * 3 * D + h * DH + t]);
    __syncthreads();
    float mx = -3e38f;
    for (int j = t; j < S; j += 256) {
        float a = 0.f;
        for (int d = 0; d < DH; ++d) a += q[d] * bf2f(qkv[j * 3 * D + D + h * DH + d]);
        if (MIMIC) a = rbf(rbf(a) * scale); else a *= scale;
        p[j] = a; mx = fmaxf(mx, a);
    }
    red[t] = mx; __syncthreads();
    for (int o2 = 128; o2 > 0; o2 >>= 1) { if (t < o2) red[t] = fmaxf(red[t], red[t + o2]); __syncthreads(); }
    mx = red[0]; __syncthreads();
    float l = 0.f;
    for (int j = t; j < S; j += 256) { float e = __expf(p[j] - mx); p[j] = e; l += e; }
    red[t] = l; __syncthreads();
    for (int o2 = 128; o2 > 0; o2 >>= 1) { if (t < o2) red[t] += red[t + o2]; __syncthreads(); }
    l = red[0]; __syncthreads();
    float rl = 1.f / l;
    int g = t >> 5, d = t & 31;
    float a = 0.f;
    if (d < DH) for (int j = g * 256; j < g * 256 + 256; ++j) { float pp = p[j] * rl; if (MIMIC) pp = rbf(pp); a += pp * bf2f(qkv[j * 3 * D + 2 * D + h * DH + d]); }
    red[t] = a; __syncthreads();
    if (t < DH) { float r = 0.f; for (int gg = 0; gg < 8; ++gg) r += red[gg * 32 + t]; o[s * D + h * DH + t] = f2bf(r); }
}

__device__ __forceinline__ void spin_amp(unsigned long long r0, unsigned long long r1, int amp) {
    unsigned long long tgt = r1 + (r1 - r0) * amp;
    while (__builtin_amdgcn_s_memrealtime() < tgt) __builtin_amdgcn_s_sleep(8);
}
typedef __attribute__((ext_vector_type(16))) float f32x16;
typedef __attribute__((ext_vector_type(8))) short bf16x8;
__global__ void __launch_bounds__(512) probe_clk(unsigned* sink, int iters) {
    f32x16 c = {}; bf16x8 a = {0x3f81,0x3e12,0x3d33,(short)0xbf44,0x3f55,0x3e66,(short)0xbd77,0x3f08}; float e = threadIdx.x * -0.01f, y = 0;
    a[0] += threadIdx.x; asm volatile("" : "+v"(a));
    unsigned long long r0 = 0;
    for (int it = 0; it < iters + 2; ++it) {
        if (it == 2) r0 = __builtin_amdgcn_s_memrealtime();
        asm volatile(".rept 8\n\tv_mfma_f32_32x32x16_bf16 %0, %2, %2, %0\n\tv_exp_f32 %1, %3\n\tv_exp_f32 %1, %3\n\tv_exp_f32 %1, %3\n\tv_exp_f32 %1, %3\n\t.endr" : "+v"(c), "+v"(y) : "v"(a), "v"(e));
    }
    unsigned long long r1 = __builtin_amdgcn_s_memrealtime();
    spin_amp(r0, r1, 500);
    asm volatile("s_nop 15\n\ts_nop 15");
    if (c[3] + y == 1.2345f) sink[0] = 1;
}
__global__ void probe_karg(unsigned* sink, unsigned long long x) {
    unsigned long long r0, r1, v;
    asm volatile("s_memrealtime %0\n\ts_waitcnt lgkmcnt(0)\n\ts_load_dwordx2 %2, %3, 0x8\n\ts_waitcnt lgkmcnt(0)\n\ts_memrealtime %1\n\ts_waitcnt lgkmcnt(0)" : "=&s"(r0), "=&s"(r1), "=&s"(v) : "s"(__builtin_amdgcn_kernarg_segment_ptr()));
    spin_amp(r0, r1, 2000);
    if (v == 12345) __builtin_trap();
}
__global__ void probe_jump(unsigned* sink) {
    unsigned long long r0, r1;
    asm volatile("s_memrealtime %0\n\ts_waitcnt lgkmcnt(0)\n\t.rept 4\n\ts_branch 1024\n\t.fill 1024, 4, 0xbf800000\n\t.endr\n\ts_memrealtime %1\n\ts_waitcnt lgkmcnt(0)" : "=&s"(r0), "=&s"(r1));
    spin_amp(r0, r1, 1000);
}
__global__ void probe_nop(unsigned* sink) {
    unsigned long long r0, r1;
    asm volatile("s_memrealtime %0\n\ts_waitcnt lgkmcnt(0)\n\t.fill 4096, 4, 0xbf800000\n\ts_memrealtime %1\n\ts_waitcnt lgkmcnt(0)" : "=&s"(r0), "=&s"(r1));
    spin_amp(r0, r1, 300);
}
__global__ void probe_cal(unsigned* sink) {
    unsigned long long r0 = __builtin_amdgcn_s_memrealtime();
    spin_amp(r0, r0 + 100, 2999);
}
__global__ void probe_jump2(unsigned* sink) {
    unsigned long long r0, r1; unsigned t; unsigned long long pc;
    unsigned off = threadIdx.x * 128;
    asm volatile("s_getpc_b64 %3\n\tglobal_load_dword %2, %4, %3\n\ts_waitcnt vmcnt(0)\n\ts_barrier\n\ts_memrealtime %0\n\ts_waitcnt lgkmcnt(0)\n\t.rept 4\n\ts_branch 1024\n\t.fill 1024, 4, 0xbf800000\n\t.endr\n\ts_memrealtime %1\n\ts_waitcnt lgkmcnt(0)\n\ts_branch 4096\n\t.fill 4096, 4, 0xbf800000" : "=&s"(r0), "=&s"(r1), "=&v"(t), "=&s"(pc) : "v"(off));
    spin_amp(r0, r1, 1000);
}

extern "C" void kernel_launch(void* const* d_in, const int* in_sizes, int n_in,
                              void* d_out, int out_size, void* d_ws, size_t ws_size,
                              hipStream_t stream) {
    const u16* xA = (const u16*)d_in[0]; const u16* wqkvA = (const u16*)d_in[1]; const u16* woA = (const u16*)d_in[2];
    const u16* xB = (const u16*)d_in[3]; const u16* wqkvB = (const u16*)d_in[4]; const u16* woB = (const u16*)d_in[5];
    u16* out = (u16*)d_out;
    u16* qkvA = (u16*)d_ws; u16* qkvB = qkvA + S * 3 * D; u16* oA = qkvB + S * 3 * D; u16* oB = oA + S * D;
    probe_jump2<<<256, 192, 0, stream>>>((unsigned*)d_ws);
    gemm_naive<<<dim3((3 * D + 255) / 256, S), 256, 0, stream>>>(xA, wqkvA, qkvA, S, 3 * D, D, 3 * D);
    gemm_naive<<<dim3((3 * D + 255) / 256, S), 256, 0, stream>>>(xB, wqkvB, qkvB, S, 3 * D, D, 3 * D);
    attnA_naive<<<S * H / 256, 256, 0, stream>>>(qkvA, oA);
    attnB_naive<<<S * H, 256, 0, stream>>>(qkvB, oB);
    gemm_naive<<<dim3((D + 255) / 256, S), 256, 0, stream>>>(oA, woA, out, S, D, D, 2 * D);
    gemm_naive<<<dim3((D + 255) / 256, S), 256, 0, stream>>>(oB, woB, out + D, S, D, D, 2 * D);
}
```

```cpp
#include <hip/hip_runtime.h>
#include <hip/hip_bf16.h>

__global__ void __launch_bounds__(512, 2) fused_fwd(const void* p0, unsigned n0, unsigned f0, const void* p1, unsigned n1, unsigned f1, const void* p2, unsigned n2, unsigned f2, const void* p3, unsigned n3, unsigned f3, const void* p4, unsigned n4, unsigned f4, const void* p5, unsigned n5, unsigned f5, const void* p6, unsigned n6, unsigned f6, const void* p7, unsigned n7, unsigned f7) {
    __shared__ __attribute__((aligned(16))) char lds[163840];
    auto ka = __builtin_amdgcn_kernarg_segment_ptr();
    unsigned bid = blockIdx.x, tid = threadIdx.x;
    asm volatile(
"s_load_dwordx16 s[4:19], s[0:1], 0x0\n"
"s_load_dwordx16 s[20:35], s[0:1], 0x40\n"
"s_lshr_b32 s40, s2, 3\n"
"v_lshrrev_b32 v5, 6, v0\n"
"v_and_b32 v1, 63, v0\n"
"s_getpc_b64 s[50:51]\n"
".Lfused_fwd_afterpc:\n"
"v_lshl_add_u32 v6, v0, 5, s40\n"
"v_lshlrev_b32 v6, 7, v6\n"
"v_cmp_gt_u32 vcc, .Lfused_fwd_end-.Lfused_fwd_afterpc, v6\n"
"s_and_b64 exec, exec, vcc\n"
"s_cbranch_execz .Lfused_fwd_nowarm\n"
"global_load_dword v255, v6, s[50:51]\n"
".Lfused_fwd_nowarm:\n"
"s_mov_b64 exec, -1\n"
"v_readfirstlane_b32 s38, v5\n"
"s_and_b32 s39, s2, 7\n"
"s_lshr_b32 s50, s39, 1\n"
"s_mul_i32 s51, s50, 30\n"
"s_add_u32 s51, s51, s40\n"
"s_mul_i32 s52, s51, 187\n"
"s_lshr_b32 s52, s52, 11\n"
"s_mul_i32 s53, s52, 11\n"
"s_sub_u32 s53, s51, s53\n"
"s_and_b32 s54, s39, 1\n"
"s_add_u32 s55, s53, s54\n"
"s_cmp_ge_u32 s55, 6\n"
"s_cselect_b32 s55, 1, 0\n"
"s_lshl_b32 s41, s52, 1\n"
"s_add_u32 s41, s41, s55\n"
"s_sub_i32 s54, s54, s55\n"
"s_mul_i32 s54, s54, 6\n"
"s_add_i32 s42, s53, s54\n"
"s_cmp_lt_u32 s40, 30\n"
"s_cselect_b32 s63, 1, 0\n"
"s_cmp_eq_u32 s51, 120\n"
"s_cselect_b32 s63, 1, s63\n"
"v_lshlrev_b32 v4, 4, v1\n"
"v_lshrrev_b32 v2, 5, v1\n"
"v_and_b32 v3, 31, v1\n"
"s_cmp_eq_u32 s63, 0\n"
"s_cbranch_scc1 .Lfused_fwd_idle\n"
"s_cmp_lt_u32 s38, 4\n"
"s_cbranch_scc1 .Lfused_fwd_gcomp\n"
"s_lshl_b32 s50, s50, 1\n"
"s_add_u32 s50, s50, s42\n"
"s_sub_u32 s51, s50, 7\n"
"s_cmp_ge_u32 s50, 7\n"
"s_cselect_b32 s50, s51, s50\n"
"s_sub_u32 s51, s50, 7\n"
"s_cmp_ge_u32 s50, 7\n"
"s_cselect_b32 s50, s51, s50\n"
"s_sub_u32 s55, s38, 4\n"
"s_mul_i32 s51, s41, 96\n"
"s_lshl_b32 s52, s55, 3\n"
"s_add_u32 s51, s51, s52\n"
"v_lshrrev_b32 v5, 3, v1\n"
"v_add_u32 v5, s51, v5\n"
"v_mul_u32_u24 v5, 896, v5\n"
"s_and_b32 s52, s55, 1\n"
"s_lshl_b32 s52, s52, 2\n"
"v_lshrrev_b32 v6, 4, v1\n"
"v_add_u32 v7, s52, v6\n"
"v_and_b32 v8, 7, v1\n"
"v_xor_b32 v7, v7, v8\n"
"v_lshl_add_u32 v140, v7, 4, v5\n"
"v_add_u32 v141, 28672, v140\n"
"v_add_u32 v142, 57344, v140\n"
"s_lshl_b32 s52, s55, 2\n"
"v_add_u32 v5, s52, v6\n"
"v_mul_u32_u24 v5, 2688, v5\n"
"v_lshlrev_b32 v6, 2, v6\n"
"v_and_b32 v8, 15, v1\n"
"v_xor_b32 v6, v6, v8\n"
"v_lshl_add_u32 v5, v6, 4, v5\n"
"s_lshl_b32 s52, s42, 8\n"
"v_add_u32 v143, s52, v5\n"
"s_cmp_eq_u32 s42, 10\n"
"s_cselect_b32 s52, 8, 16\n"
"v_cmp_gt_u32 vcc, s52, v6\n"
"v_mov_b32 v5, 2130706432\n"
"s_lshl_b32 s60, s55, 10\n"
"v_cndmask_b32 v143, v5, v143, vcc\n"
"v_add_u32 v144, 43008, v143\n"
"v_add_u32 v145, 86016, v143\n"
"v_add_u32 v146, 129024, v143\n"
"s_lshl_b32 s58, s50, 7\n"
"s_mul_i32 s59, s50, 172032\n"
"s_waitcnt lgkmcnt(0)\n"
"s_add_u32 m0, s60, 118784\n"
"s_nop 0\n"
"buffer_load_dwordx4 v143, s[8:11], s59 offen lds\n"
"s_add_u32 m0, s60, 106496\n"
"s_nop 0\n"
"buffer_load_dwordx4 v140, s[4:7], s58 offen lds\n"
"s_add_u32 m0, s60, 122880\n"
"s_nop 0\n"
"buffer_load_dwordx4 v144, s[8:11], s59 offen lds\n"
"s_add_u32 m0, s60, 110592\n"
"s_nop 0\n"
"buffer_load_dwordx4 v141, s[4:7], s58 offen lds\n"
"s_add_u32 m0, s60, 126976\n"
"s_nop 0\n"
"buffer_load_dwordx4 v145, s[8:11], s59 offen lds\n"
"s_add_u32 m0, s60, 114688\n"
"s_nop 0\n"
"buffer_load_dwordx4 v142, s[4:7], s58 offen lds\n"
"s_add_u32 m0, s60, 131072\n"
"s_nop 0\n"
"buffer_load_dwordx4 v146, s[8:11], s59 offen lds\n"
"s_add_u32 s58, s58, 128\n"
"s_add_u32 s59, s59, 172032\n"
"s_cmp_eq_u32 s58, 896\n"
"s_cselect_b32 s58, 0, s58\n"
"s_cselect_b32 s59, 0, s59\n"
"s_add_u32 m0, s60, 147456\n"
"s_nop 0\n"
"buffer_load_dwordx4 v143, s[8:11], s59 offen lds\n"
"s_add_u32 m0, s60, 135168\n"
"s_nop 0\n"
"buffer_load_dwordx4 v140, s[4:7], s58 offen lds\n"
"s_add_u32 m0, s60, 151552\n"
"s_nop 0\n"
"buffer_load_dwordx4 v144, s[8:11], s59 offen lds\n"
"s_add_u32 m0, s60, 139264\n"
"s_nop 0\n"
"buffer_load_dwordx4 v141, s[4:7], s58 offen lds\n"
"s_add_u32 m0, s60, 155648\n"
"s_nop 0\n"
"buffer_load_dwordx4 v145, s[8:11], s59 offen lds\n"
"s_add_u32 m0, s60, 143360\n"
"s_nop 0\n"
"buffer_load_dwordx4 v142, s[4:7], s58 offen lds\n"
"s_add_u32 m0, s60, 159744\n"
"s_nop 0\n"
"buffer_load_dwordx4 v146, s[8:11], s59 offen lds\n"
"s_add_u32 s58, s58, 128\n"
"s_add_u32 s59, s59, 172032\n"
"s_cmp_eq_u32 s58, 896\n"
"s_cselect_b32 s58, 0, s58\n"
"s_cselect_b32 s59, 0, s59\n"
"s_add_u32 s50, s20, 16777216\n"
"s_addc_u32 s51, s21, 0\n"
"s_load_dwordx2 s[36:37], s[50:51], 0x0\n"
"s_and_b32 s43, s40, 1\n"
"s_lshr_b32 s44, s40, 1\n"
"s_mul_i32 s47, s39, 3584\n"
"s_mul_i32 s52, s43, 1536\n"
"s_add_u32 s47, s47, s52\n"
"s_lshr_b32 s45, s38, 2\n"
"s_and_b32 s46, s38, 3\n"
"s_lshl_b32 s48, s38, 10\n"
"s_lshl_b32 s52, s44, 2\n"
"s_add_u32 s52, s52, s46\n"
"s_mul_i32 s49, s52, 28672\n"
"s_add_u32 s49, s49, s47\n"
"s_mov_b32 s70, 344064\n"
"s_mov_b32 s71, 401408\n"
"s_mov_b32 s72, 458752\n"
"s_mov_b32 s73, 516096\n"
"s_mov_b32 s74, 573440\n"
"s_mov_b32 s75, 630784\n"
"s_mov_b32 s76, 688128\n"
"s_mov_b32 s77, 745472\n"
"s_mov_b32 s78, 802816\n"
"s_mov_b32 s79, 860160\n"
"s_mov_b32 s80, 917504\n"
"s_mov_b32 s81, 974848\n"
"s_mov_b32 s82, 1032192\n"
"s_mov_b32 s83, 1089536\n"
"s_mov_b32 s84, 1146880\n"
"s_mov_b32 s85, 1204224\n"
"s_mov_b32 s86, 1261568\n"
"s_mov_b32 s87, 1318912\n"
"s_mov_b32 s88, 1376256\n"
"s_mov_b32 s89, 1433600\n"
"s_mov_b32 s90, 1490944\n"
"s_mov_b32 s91, 1548288\n"
"s_mov_b32 s92, 1605632\n"
"s_mov_b32 s93, 1662976\n"
"s_mov_b32 s94, 1720320\n"
"s_mov_b32 s95, 1777664\n"
"s_lshr_b32 s52, s47, 2\n"
"v_mul_u32_u24 v5, 7168, v2\n"
"v_lshl_add_u32 v5, v3, 4, v5\n"
"v_add_u32 v57, s52, v5\n"
"s_waitcnt lgkmcnt(0)\n"
"s_add_u32 s36, s36, 1\n"
"s_addc_u32 s37, s37, 0\n"
"s_waitcnt vmcnt(7)\n"
"s_barrier\n"
"s_add_u32 m0, s60, 61440\n"
"s_nop 0\n"
"buffer_load_dwordx4 v143, s[8:11], s59 offen lds\n"
"s_add_u32 m0, s60, 49152\n"
"s_nop 0\n"
"buffer_load_dwordx4 v140, s[4:7], s58 offen lds\n"
"s_add_u32 m0, s60, 65536\n"
"s_nop 0\n"
"buffer_load_dwordx4 v144, s[8:11], s59 offen lds\n"
"s_add_u32 m0, s60, 53248\n"
"s_nop 0\n"
"buffer_load_dwordx4 v141, s[4:7], s58 offen lds\n"
"s_add_u32 m0, s60, 69632\n"
"s_nop 0\n"
"buffer_load_dwordx4 v145, s[8:11], s59 offen lds\n"
"s_add_u32 m0, s60, 57344\n"
"s_nop 0\n"
"buffer_load_dwordx4 v142, s[4:7], s58 offen lds\n"
"s_add_u32 m0, s60, 73728\n"
"s_nop 0\n"
"buffer_load_dwordx4 v146, s[8:11], s59 offen lds\n"
"s_add_u32 s58, s58, 128\n"
"s_add_u32 s59, s59, 172032\n"
"s_cmp_eq_u32 s58, 896\n"
"s_cselect_b32 s58, 0, s58\n"
"s_cselect_b32 s59, 0, s59\n"
"s_lshl_b32 s64, s2, 2\n"
"s_and_b32 s65, s38, 3\n"
"s_add_u32 s64, s64, s65\n"
"s_mul_i32 s65, s64, 1280\n"
"s_mul_i32 s66, s64, 512\n"
"s_mul_i32 s64, s64, 1920\n"
"v_lshlrev_b32 v150, 7, v1\n"
"s_mov_b64 exec, 0x7fff\n"
"buffer_load_dword v151, v150, s[12:15], s64 offen\n"
"s_mov_b64 exec, 0x3ff\n"
"buffer_load_dword v152, v150, s[16:19], s65 offen\n"
"s_mov_b64 exec, 15\n"
"buffer_load_dword v153, v150, s[24:27], s66 offen\n"
"buffer_load_dword v154, v150, s[28:31], s66 offen\n"
"s_mov_b64 exec, -1\n"
"s_waitcnt vmcnt(11)\n"
"s_barrier\n"
"s_add_u32 m0, s60, 90112\n"
"s_nop 0\n"
"buffer_load_dwordx4 v143, s[8:11], s59 offen lds\n"
"s_add_u32 m0, s60, 77824\n"
"s_nop 0\n"
"buffer_load_dwordx4 v140, s[4:7], s58 offen lds\n"
"s_add_u32 m0, s60, 94208\n"
"s_nop 0\n"
"buffer_load_dwordx4 v144, s[8:11], s59 offen lds\n"
"s_add_u32 m0, s60, 81920\n"
"s_nop 0\n"
"buffer_load_dwordx4 v141, s[4:7], s58 offen lds\n"
"s_add_u32 m0, s60, 98304\n"
"s_nop 0\n"
"buffer_load_dwordx4 v145, s[8:11], s59 offen lds\n"
"s_add_u32 m0, s60, 86016\n"
"s_nop 0\n"
"buffer_load_dwordx4 v142, s[4:7], s58 offen lds\n"
"s_add_u32 m0, s60, 102400\n"
"s_nop 0\n"
"buffer_load_dwordx4 v146, s[8:11], s59 offen lds\n"
"s_add_u32 s58, s58, 128\n"
"s_add_u32 s59, s59, 172032\n"
"s_cmp_eq_u32 s58, 896\n"
"s_cselect_b32 s58, 0, s58\n"
"s_cselect_b32 s59, 0, s59\n"
"s_mov_b32 s62, s2\n"
"s_lshl_b32 s62, s62, 7\n"
"s_add_u32 s62, s62, 17039360\n"
"s_cmp_eq_u32 s38, 4\n"
"s_cselect_b32 s62, s62, 2130706432\n"
"v_mov_b32 v24, s36\n"
"v_mov_b32 v25, s37\n"
"s_mov_b64 exec, 1\n"
"buffer_store_dwordx2 v[24:25], off, s[20:23], s62 sc1\n"
"s_mov_b64 exec, -1\n"
"s_waitcnt vmcnt(12)\n"
"s_barrier\n"
"s_add_u32 m0, s60, 118784\n"
"s_nop 0\n"
"buffer_load_dwordx4 v143, s[8:11], s59 offen lds\n"
"s_add_u32 m0, s60, 106496\n"
"s_nop 0\n"
"buffer_load_dwordx4 v140, s[4:7], s58 offen lds\n"
"s_add_u32 m0, s60, 122880\n"
"s_nop 0\n"
"buffer_load_dwordx4 v144, s[8:11], s59 offen lds\n"
"s_add_u32 m0, s60, 110592\n"
"s_nop 0\n"
"buffer_load_dwordx4 v141, s[4:7], s58 offen lds\n"
"s_add_u32 m0, s60, 126976\n"
"s_nop 0\n"
"buffer_load_dwordx4 v145, s[8:11], s59 offen lds\n"
"s_add_u32 m0, s60, 114688\n"
"s_nop 0\n"
"buffer_load_dwordx4 v142, s[4:7], s58 offen lds\n"
"s_add_u32 m0, s60, 131072\n"
"s_nop 0\n"
"buffer_load_dwordx4 v146, s[8:11], s59 offen lds\n"
"s_add_u32 s58, s58, 128\n"
"s_add_u32 s59, s59, 172032\n"
"s_cmp_eq_u32 s58, 896\n"
"s_cselect_b32 s58, 0, s58\n"
"s_cselect_b32 s59, 0, s59\n"
"s_and_b32 s52, s38, 1\n"
"s_mul_i32 s53, s45, 28672\n"
"s_lshl_b32 s54, s52, 10\n"
"s_add_u32 s54, s54, s47\n"
"s_add_u32 s54, s54, 2097152\n"
"s_mul_i32 s55, s52, 14336\n"
"s_add_u32 s55, s55, 4194304\n"
"s_bitcmp1_b32 s38, 1\n"
"s_cselect_b32 s54, s55, s54\n"
"s_cselect_b64 vcc, -1, 0\n"
"s_add_u32 s54, s54, s53\n"
"v_cndmask_b32 v5, v4, v57, vcc\n"
"v_add_u32 v10, s54, v5\n"
"v_subrev_u32 v11, 28672, v10\n"
"s_lshl_b32 s52, s45, 12\n"
"v_add_u32 v12, s52, v4\n"
"s_xor_b32 s53, s43, 1\n"
"s_mul_i32 s53, s53, 28\n"
"v_subrev_u32 v7, s53, v3\n"
"v_cmp_gt_u32 vcc, 4, v7\n"
"v_add_u32 v7, 2048, v12\n"
"v_add_u32 v8, 3072, v12\n"
"v_mov_b32 v5, 8192\n"
"v_cndmask_b32 v14, v7, v5, vcc\n"
"v_cndmask_b32 v16, v8, v5, vcc\n"
"v_add_u32 v13, 57456, v12\n"
"v_add_u32 v15, 57456, v14\n"
"v_add_u32 v17, 57456, v16\n"
"v_mov_b32 v9, 0xdddd\n"
"v_mov_b32 v76, 0\n"
"v_mov_b32 v77, 0\n"
"s_waitcnt vmcnt(8)\n"
"s_barrier\n"
"s_add_u32 m0, s60, 147456\n"
"s_nop 0\n"
"buffer_load_dwordx4 v143, s[8:11], s59 offen lds\n"
"s_add_u32 m0, s60, 135168\n"
"s_nop 0\n"
"buffer_load_dwordx4 v140, s[4:7], s58 offen lds\n"
"s_add_u32 m0, s60, 151552\n"
"s_nop 0\n"
"buffer_load_dwordx4 v144, s[8:11], s59 offen lds\n"
"s_add_u32 m0, s60, 139264\n"
"s_nop 0\n"
"buffer_load_dwordx4 v141, s[4:7], s58 offen lds\n"
"s_add_u32 m0, s60, 155648\n"
"s_nop 0\n"
"buffer_load_dwordx4 v145, s[8:11], s59 offen lds\n"
"s_add_u32 m0, s60, 143360\n"
"s_nop 0\n"
"buffer_load_dwordx4 v142, s[4:7], s58 offen lds\n"
"s_add_u32 m0, s60, 159744\n"
"s_nop 0\n"
"buffer_load_dwordx4 v146, s[8:11], s59 offen lds\n"
"s_add_u32 s58, s58, 128\n"
"s_add_u32 s59, s59, 172032\n"
"s_cmp_eq_u32 s58, 896\n"
"s_cselect_b32 s58, 0, s58\n"
"s_cselect_b32 s59, 0, s59\n"
"v_mov_b32 v78, 0\n"
"v_mov_b32 v79, 0\n"
"v_mov_b32 v80, 0\n"
"v_mov_b32 v81, 0\n"
"v_mov_b32 v82, 0\n"
"v_mov_b32 v83, 0\n"
"v_mov_b32 v84, 0\n"
"v_mov_b32 v85, 0\n"
"v_mov_b32 v86, 0\n"
"v_mov_b32 v87, 0\n"
"v_mov_b32 v88, 0\n"
"v_mov_b32 v89, 0\n"
"v_mov_b32 v90, 0\n"
"v_mov_b32 v91, 0\n"
"s_lshl_b32 s52, s46, 12\n"
"s_lshl_b32 s53, s45, 11\n"
"s_add_u32 s52, s52, 82080\n"
"s_add_u32 s54, s52, s53\n"
"v_add_u32 v28, s54, v4\n"
"s_xor_b32 s53, s53, 2048\n"
"s_add_u32 s54, s52, s53\n"
"v_add_u32 v29, s54, v4\n"
"s_lshl_b32 s52, s46, 9\n"
"s_add_u32 s52, s52, 159904\n"
"v_lshl_add_u32 v31, v3, 2, s52\n"
"s_lshl_b32 s53, s45, 8\n"
"s_xor_b32 s54, s43, 1\n"
"v_xor_b32 v5, s54, v2\n"
"v_lshl_add_u32 v5, v5, 7, v31\n"
"v_add_u32 v30, s53, v5\n"
"s_waitcnt vmcnt(7)\n"
"s_barrier\n"
"s_add_u32 m0, s60, 61440\n"
"s_nop 0\n"
"buffer_load_dwordx4 v143, s[8:11], s59 offen lds\n"
"s_add_u32 m0, s60, 49152\n"
"s_nop 0\n"
"buffer_load_dwordx4 v140, s[4:7], s58 offen lds\n"
"s_add_u32 m0, s60, 65536\n"
"s_nop 0\n"
"buffer_load_dwordx4 v144, s[8:11], s59 offen lds\n"
"s_add_u32 m0, s60, 53248\n"
"s_nop 0\n"
"buffer_load_dwordx4 v141, s[4:7], s58 offen lds\n"
"s_add_u32 m0, s60, 69632\n"
"s_nop 0\n"
"buffer_load_dwordx4 v145, s[8:11], s59 offen lds\n"
"s_add_u32 m0, s60, 57344\n"
"s_nop 0\n"
"buffer_load_dwordx4 v142, s[4:7], s58 offen lds\n"
"s_add_u32 m0, s60, 73728\n"
"s_nop 0\n"
"buffer_load_dwordx4 v146, s[8:11], s59 offen lds\n"
"s_add_u32 s58, s58, 128\n"
"s_add_u32 s59, s59, 172032\n"
"s_cmp_eq_u32 s58, 896\n"
"s_cselect_b32 s58, 0, s58\n"
"s_cselect_b32 s59, 0, s59\n"
"v_lshrrev_b32 v5, 4, v1\n"
"s_lshl_b32 s52, s38, 2\n"
"v_add_u32 v6, s52, v5\n"
"v_mul_u32_u24 v6, 896, v6\n"
"s_and_b32 s52, s38, 2\n"
"s_lshl_b32 s52, s52, 2\n"
"v_lshl_add_u32 v7, v5, 1, s52\n"
"v_and_b32 v8, 15, v1\n"
"v_xor_b32 v7, v7, v8\n"
"v_lshl_add_u32 v6, v7, 4, v6\n"
"s_and_b32 s53, s40, 3\n"
"s_lshl_b32 s54, s53, 8\n"
"v_add_u32 v6, s54, v6\n"
"s_cmp_eq_u32 s53, 3\n"
"s_cselect_b32 s52, 8, 16\n"
"v_cmp_gt_u32 vcc, s52, v7\n"
"v_mov_b32 v7, 2130706432\n"
"s_lshl_b32 s55, s38, 5\n"
"v_cndmask_b32 v18, v7, v6, vcc\n"
"s_add_u32 s54, s54, s55\n"
"s_cmp_lt_u32 s54, 896\n"
"s_cselect_b32 s54, s54, 2130706432\n"
"s_lshl_b32 s52, s39, 3\n"
"s_lshr_b32 s55, s40, 2\n"
"s_add_u32 s52, s52, s55\n"
"s_mul_i32 s55, s52, 57344\n"
"s_add_u32 s54, s54, s55\n"
"s_add_u32 s54, s54, 896\n"
"v_mul_u32_u24 v6, 1792, v3\n"
"v_lshl_add_u32 v6, v2, 4, v6\n"
"v_add_u32 v58, s54, v6\n"
"s_waitcnt vmcnt(7)\n"
"s_barrier\n"
"s_add_u32 m0, s60, 90112\n"
"s_nop 0\n"
"buffer_load_dwordx4 v143, s[16:19], s59 offen lds\n"
"s_add_u32 m0, s60, 77824\n"
"s_nop 0\n"
"buffer_load_dwordx4 v140, s[12:15], s58 offen lds\n"
"s_add_u32 m0, s60, 94208\n"
"s_nop 0\n"
"buffer_load_dwordx4 v144, s[16:19], s59 offen lds\n"
"s_add_u32 m0, s60, 81920\n"
"s_nop 0\n"
"buffer_load_dwordx4 v141, s[12:15], s58 offen lds\n"
"s_add_u32 m0, s60, 98304\n"
"s_nop 0\n"
"buffer_load_dwordx4 v145, s[16:19], s59 offen lds\n"
"s_add_u32 m0, s60, 86016\n"
"s_nop 0\n"
"buffer_load_dwordx4 v142, s[12:15], s58 offen lds\n"
"s_add_u32 m0, s60, 102400\n"
"s_nop 0\n"
"buffer_load_dwordx4 v146, s[16:19], s59 offen lds\n"
"s_add_u32 s58, s58, 128\n"
"s_add_u32 s59, s59, 172032\n"
"s_cmp_eq_u32 s58, 896\n"
"s_cselect_b32 s58, 0, s58\n"
"s_cselect_b32 s59, 0, s59\n"
"v_bfe_u32 v7, v1, 2, 2\n"
"v_lshl_add_u32 v5, v5, 3, v7\n"
"v_lshlrev_b32 v5, 8, v5\n"
"v_bfe_u32 v7, v1, 2, 3\n"
"v_xor_b32 v7, s38, v7\n"
"v_lshl_add_u32 v5, v7, 5, v5\n"
"v_and_b32 v7, 3, v1\n"
"v_lshl_add_u32 v5, v7, 3, v5\n"
"v_add_u32 v20, 82080, v5\n"
"v_add_u32 v21, 131232, v5\n"
"s_min_u32 s53, s40, 27\n"
"s_mul_i32 s54, s53, 37\n"
"s_lshr_b32 s54, s54, 8\n"
"s_mul_i32 s55, s54, 7\n"
"s_sub_u32 s53, s53, s55\n"
"s_lshl_b32 s55, s39, 2\n"
"s_add_u32 s54, s54, s55\n"
"s_lshl_b32 s53, s53, 7\n"
"v_lshrrev_b32 v5, 3, v1\n"
"s_lshl_b32 s55, s38, 3\n"
"v_add_u32 v5, s55, v5\n"
"v_mul_u32_u24 v5, 896, v5\n"
"v_bfe_u32 v7, v1, 4, 1\n"
"v_lshlrev_b32 v7, 2, v7\n"
"v_and_b32 v8, 7, v1\n"
"v_xor_b32 v7, v7, v8\n"
"v_lshl_add_u32 v5, v7, 4, v5\n"
"v_add_u32 v19, s53, v5\n"
"s_lshl_b32 s55, s54, 1\n"
"s_and_b32 s50, s38, 1\n"
"s_waitcnt vmcnt(7)\n"
"s_barrier\n"
"s_add_u32 m0, s60, 118784\n"
"s_nop 0\n"
"buffer_load_dwordx4 v143, s[16:19], s59 offen lds\n"
"s_add_u32 m0, s60, 106496\n"
"s_nop 0\n"
"buffer_load_dwordx4 v140, s[12:15], s58 offen lds\n"
"s_add_u32 m0, s60, 122880\n"
"s_nop 0\n"
"buffer_load_dwordx4 v144, s[16:19], s59 offen lds\n"
"s_add_u32 m0, s60, 110592\n"
"s_nop 0\n"
"buffer_load_dwordx4 v141, s[12:15], s58 offen lds\n"
"s_add_u32 m0, s60, 126976\n"
"s_nop 0\n"
"buffer_load_dwordx4 v145, s[16:19], s59 offen lds\n"
"s_add_u32 m0, s60, 114688\n"
"s_nop 0\n"
"buffer_load_dwordx4 v142, s[12:15], s58 offen lds\n"
"s_add_u32 m0, s60, 131072\n"
"s_nop 0\n"
"buffer_load_dwordx4 v146, s[16:19], s59 offen lds\n"
"s_add_u32 s58, s58, 128\n"
"s_add_u32 s59, s59, 172032\n"
"s_cmp_eq_u32 s58, 896\n"
"s_cselect_b32 s58, 0, s58\n"
"s_cselect_b32 s59, 0, s59\n"
"s_add_u32 s55, s55, s50\n"
"s_mul_i32 s55, s55, 57344\n"
"s_add_u32 s55, s55, s53\n"
"s_and_b32 s50, s38, 2\n"
"s_lshl_b32 s50, s50, 5\n"
"s_add_u32 s55, s55, s50\n"
"v_add_u32 v59, s55, v6\n"
"s_lshl_b32 s61, s52, 8\n"
"s_add_u32 s61, s61, s54\n"
"s_lshr_b32 s50, s47, 11\n"
"s_add_u32 s51, s47, 1536\n"
"s_lshr_b32 s51, s51, 11\n"
"s_sub_u32 s51, s51, s50\n"
"v_mul_u32_u24 v5, s51, v2\n"
"v_add_u32 v5, s50, v5\n"
"s_lshl_b32 s50, s44, 2\n"
"v_min_u32 v6, 3, v3\n"
"v_add_u32 v6, s50, v6\n"
"v_mul_u32_u24 v6, 43, v6\n"
"v_lshrrev_b32 v6, 7, v6\n"
"v_min_u32 v7, 21, v3\n"
"s_add_u32 s53, s46, 1\n"
"s_mul_i32 s53, s53, 14\n"
"s_cmp_lt_u32 s46, 2\n"
"s_cselect_b32 s53, s53, 0\n"
"s_cselect_b64 vcc, -1, 0\n"
"v_cndmask_b32 v6, v6, v7, vcc\n"
"v_add_u32 v7, s53, v5\n"
"v_lshrrev_b32 v7, 2, v7\n"
"v_lshl_add_u32 v6, v7, 5, v6\n"
"v_lshlrev_b32 v6, 7, v6\n"
"s_waitcnt vmcnt(7)\n"
"s_barrier\n"
"s_add_u32 m0, s60, 147456\n"
"s_nop 0\n"
"buffer_load_dwordx4 v143, s[16:19], s59 offen lds\n"
"s_add_u32 m0, s60, 135168\n"
"s_nop 0\n"
"buffer_load_dwordx4 v140, s[12:15], s58 offen lds\n"
"s_add_u32 m0, s60, 151552\n"
"s_nop 0\n"
"buffer_load_dwordx4 v144, s[16:19], s59 offen lds\n"
"s_add_u32 m0, s60, 139264\n"
"s_nop 0\n"
"buffer_load_dwordx4 v141, s[12:15], s58 offen lds\n"
"s_add_u32 m0, s60, 155648\n"
"s_nop 0\n"
"buffer_load_dwordx4 v145, s[16:19], s59 offen lds\n"
"s_add_u32 m0, s60, 143360\n"
"s_nop 0\n"
"buffer_load_dwordx4 v142, s[12:15], s58 offen lds\n"
"s_add_u32 m0, s60, 159744\n"
"s_nop 0\n"
"buffer_load_dwordx4 v146, s[16:19], s59 offen lds\n"
"s_add_u32 s58, s58, 128\n"
"s_add_u32 s59, s59, 172032\n"
"s_cmp_eq_u32 s58, 896\n"
"s_cselect_b32 s58, 0, s58\n"
"s_cselect_b32 s59, 0, s59\n"
"v_add_u32 v22, 16842752, v6\n"
"v_min_u32 v6, 4, v3\n"
"v_add_u32 v6, s50, v6\n"
"v_max_u32 v6, 1, v6\n"
"v_subrev_u32 v6, 1, v6\n"
"v_mul_u32_u24 v6, 43, v6\n"
"v_lshrrev_b32 v6, 7, v6\n"
"s_mul_i32 s53, s46, 14\n"
"v_add_u32 v7, s53, v5\n"
"v_lshrrev_b32 v7, 2, v7\n"
"v_lshl_add_u32 v6, v7, 5, v6\n"
"v_lshlrev_b32 v6, 7, v6\n"
"v_add_u32 v6, 16908288, v6\n"
"v_min_u32 v7, 13, v1\n"
"v_lshlrev_b32 v7, 7, v7\n"
"v_add_u32 v7, 16973824, v7\n"
"s_cmp_eq_u32 s46, 3\n"
"s_cselect_b64 vcc, -1, 0\n"
"v_cndmask_b32 v23, v6, v7, vcc\n"
"s_add_u32 s52, s50, s46\n"
"v_mov_b32 v220, 0x5555\n"
"v_mov_b32 v221, 0\n"
"v_add_u32 v6, 18, v3\n"
"v_lshlrev_b64 v[220:221], v6, v[220:221]\n"
"s_cmp_eq_u32 s52, 0\n"
"s_cselect_b32 s53, 0, -1\n"
"v_and_b32 v220, s53, v220\n"
"v_lshlrev_b32 v6, 2, v2\n"
"v_lshrrev_b32 v220, v6, v220\n"
"v_lshrrev_b32 v221, v6, v221\n"
"s_waitcnt vmcnt(7)\n"
"s_barrier\n"
"s_lshl_b32 s62, s42, 5\n"
"s_add_u32 s62, s62, s41\n"
"s_lshl_b32 s62, s62, 7\n"
"s_add_u32 s62, s62, 16842752\n"
"s_cmp_eq_u32 s38, 4\n"
"s_cselect_b32 s62, s62, 2130706432\n"
"s_barrier\n"
"v_mov_b32 v24, s36\n"
"v_mov_b32 v25, s37\n"
"s_mov_b64 exec, 1\n"
"buffer_store_dwordx2 v[24:25], off, s[20:23], s62 sc1\n"
"s_mov_b64 exec, -1\n"
"s_add_u32 m0, s60, 61440\n"
"s_nop 0\n"
"buffer_load_dwordx4 v143, s[16:19], s59 offen lds\n"
"s_add_u32 m0, s60, 49152\n"
"s_nop 0\n"
"buffer_load_dwordx4 v140, s[12:15], s58 offen lds\n"
"s_add_u32 m0, s60, 65536\n"
"s_nop 0\n"
"buffer_load_dwordx4 v144, s[16:19], s59 offen lds\n"
"s_add_u32 m0, s60, 53248\n"
"s_nop 0\n"
"buffer_load_dwordx4 v141, s[12:15], s58 offen lds\n"
"s_add_u32 m0, s60, 69632\n"
"s_nop 0\n"
"buffer_load_dwordx4 v145, s[16:19], s59 offen lds\n"
"s_add_u32 m0, s60, 57344\n"
"s_nop 0\n"
"buffer_load_dwordx4 v142, s[12:15], s58 offen lds\n"
"s_add_u32 m0, s60, 73728\n"
"s_nop 0\n"
"buffer_load_dwordx4 v146, s[16:19], s59 offen lds\n"
"s_add_u32 s58, s58, 128\n"
"s_add_u32 s59, s59, 172032\n"
"s_cmp_eq_u32 s58, 896\n"
"s_cselect_b32 s58, 0, s58\n"
"s_cselect_b32 s59, 0, s59\n"
"v_not_b32 v220, v220\n"
"v_not_b32 v221, v221\n"
"v_bfe_i32 v196, v221, 0, 1\n"
"v_and_b32 v196, 0xff800000, v196\n"
"v_bfe_i32 v197, v221, 1, 1\n"
"v_and_b32 v197, 0xff800000, v197\n"
"v_bfe_i32 v198, v221, 2, 1\n"
"v_and_b32 v198, 0xff800000, v198\n"
"v_bfe_i32 v199, v221, 3, 1\n"
"v_and_b32 v199, 0xff800000, v199\n"
"v_bfe_i32 v200, v221, 8, 1\n"
"v_and_b32 v200, 0xff800000, v200\n"
"v_bfe_i32 v201, v221, 9, 1\n"
"v_and_b32 v201, 0xff800000, v201\n"
"v_bfe_i32 v202, v221, 10, 1\n"
"v_and_b32 v202, 0xff800000, v202\n"
"v_bfe_i32 v203, v221, 11, 1\n"
"v_and_b32 v203, 0xff800000, v203\n"
"v_bfe_i32 v204, v221, 16, 1\n"
"v_and_b32 v204, 0xff800000, v204\n"
"v_bfe_i32 v205, v221, 17, 1\n"
"v_and_b32 v205, 0xff800000, v205\n"
"v_bfe_i32 v206, v221, 18, 1\n"
"v_and_b32 v206, 0xff800000, v206\n"
"v_bfe_i32 v207, v221, 19, 1\n"
"v_and_b32 v207, 0xff800000, v207\n"
"v_bfe_i32 v208, v221, 24, 1\n"
"v_and_b32 v208, 0xff800000, v208\n"
"v_bfe_i32 v209, v221, 25, 1\n"
"v_and_b32 v209, 0xff800000, v209\n"
"v_bfe_i32 v210, v221, 26, 1\n"
"s_waitcnt vmcnt(8)\n"
"s_barrier\n"
"s_add_u32 m0, s60, 90112\n"
"s_nop 0\n"
"buffer_load_dwordx4 v143, s[16:19], s59 offen lds\n"
"s_add_u32 m0, s60, 77824\n"
"s_nop 0\n"
"buffer_load_dwordx4 v140, s[12:15], s58 offen lds\n"
"s_add_u32 m0, s60, 94208\n"
"s_nop 0\n"
"buffer_load_dwordx4 v144, s[16:19], s59 offen lds\n"
"s_add_u32 m0, s60, 81920\n"
"s_nop 0\n"
"buffer_load_dwordx4 v141, s[12:15], s58 offen lds\n"
"s_add_u32 m0, s60, 98304\n"
"s_nop 0\n"
"buffer_load_dwordx4 v145, s[16:19], s59 offen lds\n"
"s_add_u32 m0, s60, 86016\n"
"s_nop 0\n"
"buffer_load_dwordx4 v142, s[12:15], s58 offen lds\n"
"s_add_u32 m0, s60, 102400\n"
"s_nop 0\n"
"buffer_load_dwordx4 v146, s[16:19], s59 offen lds\n"
"s_add_u32 s58, s58, 128\n"
"s_add_u32 s59, s59, 172032\n"
"s_cmp_eq_u32 s58, 896\n"
"s_cselect_b32 s58, 0, s58\n"
"s_cselect_b32 s59, 0, s59\n"
"v_and_b32 v210, 0xff800000, v210\n"
"v_bfe_i32 v211, v221, 27, 1\n"
"v_and_b32 v211, 0xff800000, v211\n"
"v_bfe_i32 v212, v220, 16, 1\n"
"v_and_b32 v212, 0xff800000, v212\n"
"v_bfe_i32 v213, v220, 17, 1\n"
"v_and_b32 v213, 0xff800000, v213\n"
"v_bfe_i32 v214, v220, 18, 1\n"
"v_and_b32 v214, 0xff800000, v214\n"
"v_bfe_i32 v215, v220, 19, 1\n"
"v_and_b32 v215, 0xff800000, v215\n"
"v_bfe_i32 v216, v220, 24, 1\n"
"v_and_b32 v216, 0xff800000, v216\n"
"v_bfe_i32 v217, v220, 25, 1\n"
"v_and_b32 v217, 0xff800000, v217\n"
"v_bfe_i32 v218, v220, 26, 1\n"
"v_and_b32 v218, 0xff800000, v218\n"
"v_bfe_i32 v219, v220, 27, 1\n"
"v_and_b32 v219, 0xff800000, v219\n"
"v_mov_b32 v244, 0x3f803f80\n"
"v_mov_b32 v245, 0x3f803f80\n"
"v_mov_b32 v246, 0x3f803f80\n"
"v_mov_b32 v247, 0x3f803f80\n"
"v_mov_b32 v248, 0x3f803f80\n"
"v_mov_b32 v249, 0x3f803f80\n"
"v_mov_b32 v250, 0x3f803f80\n"
"v_mov_b32 v251, 0x3f803f80\n"
"v_mov_b32 v252, 0x3f803f80\n"
"v_mov_b32 v253, 0x3f803f80\n"
"v_mov_b32 v254, 0x3f803f80\n"
"v_mov_b32 v255, 0x3f803f80\n"
"s_waitcnt vmcnt(7)\n"
"s_barrier\n"
"s_add_u32 m0, s60, 118784\n"
"s_nop 0\n"
"buffer_load_dwordx4 v143, s[16:19], s59 offen lds\n"
"s_add_u32 m0, s60, 106496\n"
"s_nop 0\n"
"buffer_load_dwordx4 v140, s[12:15], s58 offen lds\n"
"s_add_u32 m0, s60, 122880\n"
"s_nop 0\n"
"buffer_load_dwordx4 v144, s[16:19], s59 offen lds\n"
"s_add_u32 m0, s60, 110592\n"
"s_nop 0\n"
"buffer_load_dwordx4 v141, s[12:15], s58 offen lds\n"
"s_add_u32 m0, s60, 126976\n"
"s_nop 0\n"
"buffer_load_dwordx4 v145, s[16:19], s59 offen lds\n"
"s_add_u32 m0, s60, 114688\n"
"s_nop 0\n"
"buffer_load_dwordx4 v142, s[12:15], s58 offen lds\n"
"s_add_u32 m0, s60, 131072\n"
"s_nop 0\n"
"buffer_load_dwordx4 v146, s[16:19], s59 offen lds\n"
"s_add_u32 s58, s58, 128\n"
"s_add_u32 s59, s59, 172032\n"
"s_cmp_eq_u32 s58, 896\n"
"s_cselect_b32 s58, 0, s58\n"
"s_cselect_b32 s59, 0, s59\n"
"s_waitcnt vmcnt(7)\n"
"s_barrier\n"
"buffer_load_dwordx2 v[26:27], v22, s[20:23], 0 offen sc1\n"
"s_add_u32 m0, s60, 147456\n"
"s_nop 0\n"
"buffer_load_dwordx4 v143, s[16:19], s59 offen lds\n"
"s_add_u32 m0, s60, 135168\n"
"s_nop 0\n"
"buffer_load_dwordx4 v140, s[12:15], s58 offen lds\n"
"s_add_u32 m0, s60, 151552\n"
"s_nop 0\n"
"buffer_load_dwordx4 v144, s[16:19], s59 offen lds\n"
"s_add_u32 m0, s60, 139264\n"
"s_nop 0\n"
"buffer_load_dwordx4 v141, s[12:15], s58 offen lds\n"
"s_add_u32 m0, s60, 155648\n"
"s_nop 0\n"
"buffer_load_dwordx4 v145, s[16:19], s59 offen lds\n"
"s_add_u32 m0, s60, 143360\n"
"s_nop 0\n"
"buffer_load_dwordx4 v142, s[12:15], s58 offen lds\n"
"s_add_u32 m0, s60, 159744\n"
"s_nop 0\n"
"buffer_load_dwordx4 v146, s[16:19], s59 offen lds\n"
"s_waitcnt vmcnt(7)\n"
"v_cmp_ne_u64 vcc, s[36:37], v[26:27]\n"
"s_cbranch_vccnz .Lfused_fwd_spin_g\n"
".Lfused_fwd_spin_g_r:\n"
"s_barrier\n"
"s_lshl_b32 s50, s46, 11\n"
"s_add_u32 m0, s50, 82080\n"
"s_add_u32 s51, s49, 0\n"
"buffer_load_dwordx4 v4, s[20:23], s51 offen sc1 lds\n"
"buffer_load_dwordx4 v4, s[20:23], s51 offen offset:1024 sc1 lds\n"
"s_add_u32 m0, s48, 61568\n"
"s_mov_b32 s52, 0\n"
"buffer_load_dwordx4 v11, s[20:23], s52 offen sc1 lds\n"
"s_add_u32 m0, s48, 65664\n"
"s_mov_b32 s52, 0\n"
"buffer_load_dwordx4 v10, s[20:23], s52 offen sc1 lds\n"
"s_add_u32 m0, s48, 69776\n"
"s_mov_b32 s52, 57344\n"
"buffer_load_dwordx4 v11, s[20:23], s52 offen sc1 lds\n"
"s_add_u32 m0, s48, 73872\n"
"s_mov_b32 s52, 57344\n"
"buffer_load_dwordx4 v10, s[20:23], s52 offen sc1 lds\n"
"s_waitcnt vmcnt(6)\n"
"s_barrier\n"
"s_add_u32 m0, s48, -4096\n"
"s_mov_b32 s52, 114688\n"
"buffer_load_dwordx4 v11, s[20:23], s52 offen sc1 lds\n"
"s_add_u32 m0, s48, 0\n"
"s_mov_b32 s52, 114688\n"
"buffer_load_dwordx4 v10, s[20:23], s52 offen sc1 lds\n"
"s_add_u32 m0, s48, 4112\n"
"s_mov_b32 s52, 172032\n"
"buffer_load_dwordx4 v11, s[20:23], s52 offen sc1 lds\n"
"s_add_u32 m0, s48, 8208\n"
"s_mov_b32 s52, 172032\n"
"buffer_load_dwordx4 v10, s[20:23], s52 offen sc1 lds\n"
"s_add_u32 m0, s48, 12320\n"
"s_mov_b32 s52, 229376\n"
"buffer_load_dwordx4 v11, s[20:23], s52 offen sc1 lds\n"
"s_add_u32 m0, s48, 16416\n"
"s_mov_b32 s52, 229376\n"
"buffer_load_dwordx4 v10, s[20:23], s52 offen sc1 lds\n"
"s_add_u32 m0, s48, 20528\n"
"s_mov_b32 s52, 286720\n"
"buffer_load_dwordx4 v11, s[20:23], s52 offen sc1 lds\n"
"s_add_u32 m0, s48, 24624\n"
"s_mov_b32 s52, 286720\n"
"buffer_load_dwordx4 v10, s[20:23], s52 offen sc1 lds\n"
"s_branch .Lfused_fwd_pro_l\n"
".Lfused_fwd_gcomp:\n"
"v_bfe_u32 v5, v3, 1, 3\n"
"v_xor_b32 v5, v5, v2\n"
"v_lshlrev_b32 v5, 4, v5\n"
"v_lshl_add_u32 v5, v3, 7, v5\n"
"v_xor_b32 v6, 0, v5\n"
"v_add_u32 v196, 49152, v6\n"
"v_xor_b32 v6, 32, v5\n"
"v_add_u32 v197, 49152, v6\n"
"v_xor_b32 v6, 64, v5\n"
"v_add_u32 v198, 49152, v6\n"
"v_xor_b32 v6, 96, v5\n"
"v_add_u32 v199, 49152, v6\n"
"v_bfe_u32 v5, v1, 2, 2\n"
"v_lshl_add_u32 v6, v2, 3, v5\n"
"v_lshlrev_b32 v6, 8, v6\n"
"v_xor_b32 v5, s38, v5\n"
"v_lshl_add_u32 v6, v5, 6, v6\n"
"v_bfe_u32 v5, v1, 4, 1\n"
"v_lshl_add_u32 v6, v5, 5, v6\n"
"v_and_b32 v5, 3, v1\n"
"v_lshl_add_u32 v6, v5, 3, v6\n"
"v_add_u32 v200, 61440, v6\n"
"v_add_u32 v201, 57344, v196\n"
"v_add_u32 v202, 57344, v197\n"
"v_add_u32 v203, 57344, v198\n"
"v_add_u32 v204, 57344, v199\n"
"v_add_u32 v205, 57344, v200\n"
"s_lshl_b32 s50, s42, 2\n"
"s_add_u32 s50, s50, s38\n"
"s_lshl_b32 s59, s42, 5\n"
"s_add_u32 s59, s59, s41\n"
"s_lshl_b32 s59, s59, 7\n"
"s_add_u32 s59, s59, 16842752\n"
"s_cmp_lt_u32 s50, 14\n"
"s_cselect_b32 s60, 0x3e8b97f6, 1.0\n"
"s_cselect_b32 s51, 0, 1\n"
"s_cmp_lt_u32 s50, 28\n"
"s_cselect_b32 s51, s51, 2\n"
"s_cselect_b32 s53, 11, 9\n"
"s_mul_i32 s52, s51, 14\n"
"s_sub_u32 s52, s50, s52\n"
"s_lshl_b32 s52, s52, s53\n"
"s_lshl_b32 s51, s51, 21\n"
"s_add_u32 s58, s51, s52\n"
"s_mul_i32 s51, s41, 86016\n"
"s_add_u32 s58, s58, s51\n"
"s_cmp_lt_u32 s50, 42\n"
"s_cselect_b32 s58, s58, 2130706432\n"
"v_mul_u32_u24 v5, 7168, v2\n"
"v_lshl_add_u32 v5, v3, 4, v5\n"
"s_cmp_ge_u32 s42, 7\n"
"s_cselect_b64 vcc, -1, 0\n"
"s_cselect_b32 s51, 14, 1\n"
"s_lshl_b32 s51, s51, 10\n"
"v_cndmask_b32 v206, v4, v5, vcc\n"
"v_add_u32 v207, s51, v206\n"
"s_and_b32 s43, s40, 1\n"
"s_lshr_b32 s44, s40, 1\n"
"s_mul_i32 s47, s39, 3584\n"
"s_mul_i32 s52, s43, 1536\n"
"s_add_u32 s47, s47, s52\n"
"s_lshr_b32 s45, s38, 2\n"
"s_and_b32 s46, s38, 3\n"
"s_lshl_b32 s48, s38, 10\n"
"s_lshl_b32 s52, s44, 2\n"
"s_add_u32 s52, s52, s46\n"
"s_mul_i32 s49, s52, 28672\n"
"s_add_u32 s49, s49, s47\n"
"s_waitcnt lgkmcnt(0)\n"
"s_add_u32 s50, s20, 16777216\n"
"s_addc_u32 s51, s21, 0\n"
"s_load_dwordx2 s[36:37], s[50:51], 0x0\n"
"s_mov_b32 s70, 344064\n"
"s_mov_b32 s71, 401408\n"
"s_mov_b32 s72, 458752\n"
"s_mov_b32 s73, 516096\n"
"s_mov_b32 s74, 573440\n"
"s_mov_b32 s75, 630784\n"
"s_mov_b32 s76, 688128\n"
"s_mov_b32 s77, 745472\n"
"s_mov_b32 s78, 802816\n"
"s_mov_b32 s79, 860160\n"
"s_mov_b32 s80, 917504\n"
"s_mov_b32 s81, 974848\n"
"s_mov_b32 s82, 1032192\n"
"s_mov_b32 s83, 1089536\n"
"s_mov_b32 s84, 1146880\n"
"s_mov_b32 s85, 1204224\n"
"s_mov_b32 s86, 1261568\n"
"s_mov_b32 s87, 1318912\n"
"s_mov_b32 s88, 1376256\n"
"s_mov_b32 s89, 1433600\n"
"s_mov_b32 s90, 1490944\n"
"s_mov_b32 s91, 1548288\n"
"s_mov_b32 s92, 1605632\n"
"s_mov_b32 s93, 1662976\n"
"s_mov_b32 s94, 1720320\n"
"s_mov_b32 s95, 1777664\n"
"s_lshr_b32 s52, s47, 2\n"
"v_mul_u32_u24 v5, 7168, v2\n"
"v_lshl_add_u32 v5, v3, 4, v5\n"
"v_add_u32 v57, s52, v5\n"
"s_and_b32 s52, s38, 1\n"
"s_mul_i32 s53, s45, 28672\n"
"s_lshl_b32 s54, s52, 10\n"
"s_add_u32 s54, s54, s47\n"
"s_add_u32 s54, s54, 2097152\n"
"s_mul_i32 s55, s52, 14336\n"
"s_add_u32 s55, s55, 4194304\n"
"s_bitcmp1_b32 s38, 1\n"
"s_cselect_b32 s54, s55, s54\n"
"s_cselect_b64 vcc, -1, 0\n"
"s_add_u32 s54, s54, s53\n"
"v_cndmask_b32 v5, v4, v57, vcc\n"
"v_add_u32 v10, s54, v5\n"
"v_subrev_u32 v11, 28672, v10\n"
"s_lshl_b32 s52, s45, 12\n"
"v_add_u32 v12, s52, v4\n"
"s_xor_b32 s53, s43, 1\n"
"s_mul_i32 s53, s53, 28\n"
"v_subrev_u32 v7, s53, v3\n"
"v_cmp_gt_u32 vcc, 4, v7\n"
"v_add_u32 v7, 2048, v12\n"
"v_add_u32 v8, 3072, v12\n"
"v_mov_b32 v5, 8192\n"
"v_cndmask_b32 v14, v7, v5, vcc\n"
"v_cndmask_b32 v16, v8, v5, vcc\n"
"v_add_u32 v13, 57456, v12\n"
"v_add_u32 v15, 57456, v14\n"
"v_add_u32 v17, 57456, v16\n"
"v_mov_b32 v9, 0xdddd\n"
"v_mov_b32 v76, 0\n"
"v_mov_b32 v77, 0\n"
"v_mov_b32 v78, 0\n"
"v_mov_b32 v79, 0\n"
"v_mov_b32 v80, 0\n"
"v_mov_b32 v81, 0\n"
"v_mov_b32 v82, 0\n"
"v_mov_b32 v83, 0\n"
"v_mov_b32 v84, 0\n"
"v_mov_b32 v85, 0\n"
"v_mov_b32 v86, 0\n"
"v_mov_b32 v87, 0\n"
"v_mov_b32 v88, 0\n"
"v_mov_b32 v89, 0\n"
"v_mov_b32 v90, 0\n"
"v_mov_b32 v91, 0\n"
"s_lshl_b32 s52, s46, 12\n"
"s_lshl_b32 s53, s45, 11\n"
"s_add_u32 s52, s52, 82080\n"
"s_add_u32 s54, s52, s53\n"
"v_add_u32 v28, s54, v4\n"
"s_xor_b32 s53, s53, 2048\n"
"s_add_u32 s54, s52, s53\n"
"v_add_u32 v29, s54, v4\n"
"s_lshl_b32 s52, s46, 9\n"
"s_add_u32 s52, s52, 159904\n"
"v_lshl_add_u32 v31, v3, 2, s52\n"
"s_lshl_b32 s53, s45, 8\n"
"s_xor_b32 s54, s43, 1\n"
"v_xor_b32 v5, s54, v2\n"
"v_lshl_add_u32 v5, v5, 7, v31\n"
"v_add_u32 v30, s53, v5\n"
"v_lshrrev_b32 v5, 4, v1\n"
"s_lshl_b32 s52, s38, 2\n"
"v_add_u32 v6, s52, v5\n"
"v_mul_u32_u24 v6, 896, v6\n"
"s_and_b32 s52, s38, 2\n"
"s_lshl_b32 s52, s52, 2\n"
"v_lshl_add_u32 v7, v5, 1, s52\n"
"v_and_b32 v8, 15, v1\n"
"v_xor_b32 v7, v7, v8\n"
"v_lshl_add_u32 v6, v7, 4, v6\n"
"s_and_b32 s53, s40, 3\n"
"s_lshl_b32 s54, s53, 8\n"
"v_add_u32 v6, s54, v6\n"
"s_cmp_eq_u32 s53, 3\n"
"s_cselect_b32 s52, 8, 16\n"
"v_cmp_gt_u32 vcc, s52, v7\n"
"v_mov_b32 v7, 2130706432\n"
"s_lshl_b32 s55, s38, 5\n"
"v_cndmask_b32 v18, v7, v6, vcc\n"
"s_add_u32 s54, s54, s55\n"
"s_cmp_lt_u32 s54, 896\n"
"s_cselect_b32 s54, s54, 2130706432\n"
"s_lshl_b32 s52, s39, 3\n"
"s_lshr_b32 s55, s40, 2\n"
"s_add_u32 s52, s52, s55\n"
"s_mul_i32 s55, s52, 57344\n"
"s_add_u32 s54, s54, s55\n"
"s_add_u32 s54, s54, 896\n"
"v_mul_u32_u24 v6, 1792, v3\n"
"v_lshl_add_u32 v6, v2, 4, v6\n"
"v_add_u32 v58, s54, v6\n"
"v_bfe_u32 v7, v1, 2, 2\n"
"v_lshl_add_u32 v5, v5, 3, v7\n"
"v_lshlrev_b32 v5, 8, v5\n"
"v_bfe_u32 v7, v1, 2, 3\n"
"v_xor_b32 v7, s38, v7\n"
"v_lshl_add_u32 v5, v7, 5, v5\n"
"v_and_b32 v7, 3, v1\n"
"v_lshl_add_u32 v5, v7, 3, v5\n"
"v_add_u32 v20, 82080, v5\n"
"v_add_u32 v21, 131232, v5\n"
"s_min_u32 s53, s40, 27\n"
"s_mul_i32 s54, s53, 37\n"
"s_lshr_b32 s54, s54, 8\n"
"s_mul_i32 s55, s54, 7\n"
"s_sub_u32 s53, s53, s55\n"
"s_lshl_b32 s55, s39, 2\n"
"s_add_u32 s54, s54, s55\n"
"s_lshl_b32 s53, s53, 7\n"
"v_lshrrev_b32 v5, 3, v1\n"
"s_lshl_b32 s55, s38, 3\n"
"v_add_u32 v5, s55, v5\n"
"v_mul_u32_u24 v5, 896, v5\n"
"v_bfe_u32 v7, v1, 4, 1\n"
"v_lshlrev_b32 v7, 2, v7\n"
"v_and_b32 v8, 7, v1\n"
"v_xor_b32 v7, v7, v8\n"
"v_lshl_add_u32 v5, v7, 4, v5\n"
"v_add_u32 v19, s53, v5\n"
"s_lshl_b32 s55, s54, 1\n"
"s_and_b32 s50, s38, 1\n"
"s_add_u32 s55, s55, s50\n"
"s_mul_i32 s55, s55, 57344\n"
"s_add_u32 s55, s55, s53\n"
"s_and_b32 s50, s38, 2\n"
"s_lshl_b32 s50, s50, 5\n"
"s_add_u32 s55, s55, s50\n"
"v_add_u32 v59, s55, v6\n"
"s_lshl_b32 s61, s52, 8\n"
"s_add_u32 s61, s61, s54\n"
"s_waitcnt lgkmcnt(0)\n"
"s_add_u32 s36, s36, 1\n"
"s_addc_u32 s37, s37, 0\n"
"s_cmp_ge_u32 s42, 7\n"
"s_cbranch_scc1 .Lfused_fwd_gcomp_v\n"
"s_barrier\n"
"ds_read_b64_tr_b16 v[104:105], v205 offset:0\n"
"ds_read_b64_tr_b16 v[106:107], v205 offset:1024\n"
"ds_read_b128 v[92:95], v201 offset:0\n"
"ds_read_b128 v[96:99], v201 offset:4096\n"
"ds_read_b128 v[100:103], v201 offset:8192\n"
"s_waitcnt lgkmcnt(2)\n"
"v_mfma_f32_32x32x16_bf16 v[124:139], v[104:107], v[92:95], 0\n"
"ds_read_b64_tr_b16 v[120:121], v205 offset:4096\n"
"ds_read_b64_tr_b16 v[122:123], v205 offset:5120\n"
"s_waitcnt lgkmcnt(3)\n"
"v_mfma_f32_32x32x16_bf16 v[140:155], v[104:107], v[96:99], 0\n"
"ds_read_b128 v[108:111], v202 offset:0\n"
"ds_read_b128 v[112:115], v202 offset:4096\n"
"s_waitcnt lgkmcnt(4)\n"
"v_mfma_f32_32x32x16_bf16 v[156:171], v[104:107], v[100:103], 0\n"
"ds_read_b128 v[116:119], v202 offset:8192\n"
"s_waitcnt lgkmcnt(2)\n"
"v_mfma_f32_32x32x16_bf16 v[124:139], v[120:123], v[108:111], v[124:139]\n"
"ds_read_b64_tr_b16 v[104:105], v205 offset:8192\n"
"ds_read_b64_tr_b16 v[106:107], v205 offset:9216\n"
"s_waitcnt lgkmcnt(3)\n"
"v_mfma_f32_32x32x16_bf16 v[140:155], v[120:123], v[112:115], v[140:155]\n"
"ds_read_b128 v[92:95], v203 offset:0\n"
"ds_read_b128 v[96:99], v203 offset:4096\n"
"s_waitcnt lgkmcnt(4)\n"
"v_mfma_f32_32x32x16_bf16 v[156:171], v[120:123], v[116:119], v[156:171]\n"
"ds_read_b128 v[100:103], v203 offset:8192\n"
"s_waitcnt lgkmcnt(2)\n"
"v_mfma_f32_32x32x16_bf16 v[124:139], v[104:107], v[92:95], v[124:139]\n"
"ds_read_b64_tr_b16 v[120:121], v205 offset:12288\n"
"ds_read_b64_tr_b16 v[122:123], v205 offset:13312\n"
"s_waitcnt lgkmcnt(3)\n"
"v_mfma_f32_32x32x16_bf16 v[140:155], v[104:107], v[96:99], v[140:155]\n"
"ds_read_b128 v[108:111], v204 offset:0\n"
"ds_read_b128 v[112:115], v204 offset:4096\n"
"s_waitcnt lgkmcnt(4)\n"
"v_mfma_f32_32x32x16_bf16 v[156:171], v[104:107], v[100:103], v[156:171]\n"
"ds_read_b128 v[116:119], v204 offset:8192\n"
"s_waitcnt lgkmcnt(0)\n"
"v_mfma_f32_32x32x16_bf16 v[124:139], v[120:123], v[108:111], v[124:139]\n"
"s_barrier\n"
"ds_read_b64_tr_b16 v[104:105], v205 offset:28672\n"
"ds_read_b64_tr_b16 v[106:107], v205 offset:29696\n"
"v_mfma_f32_32x32x16_bf16 v[140:155], v[120:123], v[112:115], v[140:155]\n"
"ds_read_b128 v[92:95], v201 offset:28672\n"
"ds_read_b128 v[96:99], v201 offset:32768\n"
"v_mfma_f32_32x32x16_bf16 v[156:171], v[120:123], v[116:119], v[156:171]\n"
"ds_read_b128 v[100:103], v201 offset:36864\n"
"s_waitcnt lgkmcnt(2)\n"
"v_mfma_f32_32x32x16_bf16 v[124:139], v[104:107], v[92:95], v[124:139]\n"
"ds_read_b64_tr_b16 v[120:121], v205 offset:32768\n"
"ds_read_b64_tr_b16 v[122:123], v205 offset:33792\n"
"s_waitcnt lgkmcnt(3)\n"
"v_mfma_f32_32x32x16_bf16 v[140:155], v[104:107], v[96:99], v[140:155]\n"
"ds_read_b128 v[108:111], v202 offset:28672\n"
"ds_read_b128 v[112:115], v202 offset:32768\n"
"s_waitcnt lgkmcnt(4)\n"
"v_mfma_f32_32x32x16_bf16 v[156:171], v[104:107], v[100:103], v[156:171]\n"
"ds_read_b128 v[116:119], v202 offset:36864\n"
"s_waitcnt lgkmcnt(2)\n"
"v_mfma_f32_32x32x16_bf16 v[124:139], v[120:123], v[108:111], v[124:139]\n"
"ds_read_b64_tr_b16 v[104:105], v205 offset:36864\n"
"ds_read_b64_tr_b16 v[106:107], v205 offset:37888\n"
"s_waitcnt lgkmcnt(3)\n"
"v_mfma_f32_32x32x16_bf16 v[140:155], v[120:123], v[112:115], v[140:155]\n"
"ds_read_b128 v[92:95], v203 offset:28672\n"
"ds_read_b128 v[96:99], v203 offset:32768\n"
"s_waitcnt lgkmcnt(4)\n"
"v_mfma_f32_32x32x16_bf16 v[156:171], v[120:123], v[116:119], v[156:171]\n"
"ds_read_b128 v[100:103], v203 offset:36864\n"
"s_waitcnt lgkmcnt(2)\n"
"v_mfma_f32_32x32x16_bf16 v[124:139], v[104:107], v[92:95], v[124:139]\n"
"ds_read_b64_tr_b16 v[120:121], v205 offset:40960\n"
"ds_read_b64_tr_b16 v[122:123], v205 offset:41984\n"
"s_waitcnt lgkmcnt(3)\n"
"v_mfma_f32_32x32x16_bf16 v[140:155], v[104:107], v[96:99], v[140:155]\n"
"ds_read_b128 v[108:111], v204 offset:28672\n"
"ds_read_b128 v[112:115], v204 offset:32768\n"
"s_waitcnt lgkmcnt(4)\n"
"v_mfma_f32_32x32x16_bf16 v[156:171], v[104:107], v[100:103], v[156:171]\n"
"ds_read_b128 v[116:119], v204 offset:36864\n"
"s_waitcnt lgkmcnt(0)\n"
"v_mfma_f32_32x32x16_bf16 v[124:139], v[120:123], v[108:111], v[124:139]\n"
"s_barrier\n"
"ds_read_b64_tr_b16 v[104:105], v200 offset:0\n"
"ds_read_b64_tr_b16 v[106:107], v200 offset:1024\n"
"v_mfma_f32_32x32x16_bf16 v[140:155], v[120:123], v[112:115], v[140:155]\n"
"ds_read_b128 v[92:95], v196 offset:0\n"
"ds_read_b128 v[96:99], v196 offset:4096\n"
"v_mfma_f32_32x32x16_bf16 v[156:171], v[120:123], v[116:119], v[156:171]\n"
"ds_read_b128 v[100:103], v196 offset:8192\n"
"s_waitcnt lgkmcnt(2)\n"
"v_mfma_f32_32x32x16_bf16 v[124:139], v[104:107], v[92:95], v[124:139]\n"
"ds_read_b64_tr_b16 v[120:121], v200 offset:4096\n"
"ds_read_b64_tr_b16 v[122:123], v200 offset:5120\n"
"s_waitcnt lgkmcnt(3)\n"
"v_mfma_f32_32x32x16_bf16 v[140:155], v[104:107], v[96:99], v[140:155]\n"
"ds_read_b128 v[108:111], v197 offset:0\n"
"ds_read_b128 v[112:115], v197 offset:4096\n"
"s_waitcnt lgkmcnt(4)\n"
"v_mfma_f32_32x32x16_bf16 v[156:171], v[104:107], v[100:103], v[156:171]\n"
"ds_read_b128 v[116:119], v197 offset:8192\n"
"s_waitcnt lgkmcnt(2)\n"
"v_mfma_f32_32x32x16_bf16 v[124:139], v[120:123], v[108:111], v[124:139]\n"
"ds_read_b64_tr_b16 v[104:105], v200 offset:8192\n"
"ds_read_b64_tr_b16 v[106:107], v200 offset:9216\n"
"s_waitcnt lgkmcnt(3)\n"
"v_mfma_f32_32x32x16_bf16 v[140:155], v[120:123], v[112:115], v[140:155]\n"
"ds_read_b128 v[92:95], v198 offset:0\n"
"ds_read_b128 v[96:99], v198 offset:4096\n"
"s_waitcnt lgkmcnt(4)\n"
"v_mfma_f32_32x32x16_bf16 v[156:171], v[120:123], v[116:119], v[156:171]\n"
"ds_read_b128 v[100:103], v198 offset:8192\n"
"s_waitcnt lgkmcnt(2)\n"
"v_mfma_f32_32x32x16_bf16 v[124:139], v[104:107], v[92:95], v[124:139]\n"
"ds_read_b64_tr_b16 v[120:121], v200 offset:12288\n"
"ds_read_b64_tr_b16 v[122:123], v200 offset:13312\n"
"s_waitcnt lgkmcnt(3)\n"
"v_mfma_f32_32x32x16_bf16 v[140:155], v[104:107], v[96:99], v[140:155]\n"
"ds_read_b128 v[108:111], v199 offset:0\n"
"ds_read_b128 v[112:115], v199 offset:4096\n"
"s_waitcnt lgkmcnt(4)\n"
"v_mfma_f32_32x32x16_bf16 v[156:171], v[104:107], v[100:103], v[156:171]\n"
"ds_read_b128 v[116:119], v199 offset:8192\n"
"s_waitcnt lgkmcnt(0)\n"
"v_mfma_f32_32x32x16_bf16 v[124:139], v[120:123], v[108:111], v[124:139]\n"
"s_barrier\n"
"ds_read_b64_tr_b16 v[104:105], v200 offset:28672\n"
"ds_read_b64_tr_b16 v[106:107], v200 offset:29696\n"
"v_mfma_f32_32x32x16_bf16 v[140:155], v[120:123], v[112:115], v[140:155]\n"
"ds_read_b128 v[92:95], v196 offset:28672\n"
"ds_read_b128 v[96:99], v196 offset:32768\n"
"v_mfma_f32_32x32x16_bf16 v[156:171], v[120:123], v[116:119], v[156:171]\n"
"ds_read_b128 v[100:103], v196 offset:36864\n"
"s_waitcnt lgkmcnt(2)\n"
"v_mfma_f32_32x32x16_bf16 v[124:139], v[104:107], v[92:95], v[124:139]\n"
"ds_read_b64_tr_b16 v[120:121], v200 offset:32768\n"
"ds_read_b64_tr_b16 v[122:123], v200 offset:33792\n"
"s_waitcnt lgkmcnt(3)\n"
"v_mfma_f32_32x32x16_bf16 v[140:155], v[104:107], v[96:99], v[140:155]\n"
"ds_read_b128 v[108:111], v197 offset:28672\n"
"ds_read_b128 v[112:115], v197 offset:32768\n"
"s_waitcnt lgkmcnt(4)\n"
"v_mfma_f32_32x32x16_bf16 v[156:171], v[104:107], v[100:103], v[156:171]\n"
"ds_read_b128 v[116:119], v197 offset:36864\n"
"s_waitcnt lgkmcnt(2)\n"
"v_mfma_f32_32x32x16_bf16 v[124:139], v[120:123], v[108:111], v[124:139]\n"
"ds_read_b64_tr_b16 v[104:105], v200 offset:36864\n"
"ds_read_b64_tr_b16 v[106:107], v200 offset:37888\n"
"s_waitcnt lgkmcnt(3)\n"
"v_mfma_f32_32x32x16_bf16 v[140:155], v[120:123], v[112:115], v[140:155]\n"
"ds_read_b128 v[92:95], v198 offset:28672\n"
"ds_read_b128 v[96:99], v198 offset:32768\n"
"s_waitcnt lgkmcnt(4)\n"
"v_mfma_f32_32x32x16_bf16 v[156:171], v[120:123], v[116:119], v[156:171]\n"
"ds_read_b128 v[100:103], v198 offset:36864\n"
"s_waitcnt lgkmcnt(2)\n"
"v_mfma_f32_32x32x16_bf16 v[124:139], v[104:107], v[92:95], v[124:139]\n"
"ds_read_b64_tr_b16 v[120:121], v200 offset:40960\n"
"ds_read_b64_tr_b16 v[122:123], v200 offset:41984\n"
"s_waitcnt lgkmcnt(3)\n"
"v_mfma_f32_32x32x16_bf16 v[140:155], v[104:107], v[96:99], v[140:155]\n"
"ds_read_b128 v[108:111], v199 offset:28672\n"
"ds_read_b128 v[112:115], v199 offset:32768\n"
"s_waitcnt lgkmcnt(4)\n"
"v_mfma_f32_32x32x16_bf16 v[156:171], v[104:107], v[100:103], v[156:171]\n"
"ds_read_b128 v[116:119], v199 offset:36864\n"
"s_waitcnt lgkmcnt(0)\n"
"v_mfma_f32_32x32x16_bf16 v[124:139], v[120:123], v[108:111], v[124:139]\n"
"s_barrier\n"
"ds_read_b64_tr_b16 v[104:105], v205 offset:0\n"
"ds_read_b64_tr_b16 v[106:107], v205 offset:1024\n"
"v_mfma_f32_32x32x16_bf16 v[140:155], v[120:123], v[112:115], v[140:155]\n"
"ds_read_b128 v[92:95], v201 offset:0\n"
"ds_read_b128 v[96:99], v201 offset:4096\n"
"v_mfma_f32_32x32x16_bf16 v[156:171], v[120:123], v[116:119], v[156:171]\n"
"ds_read_b128 v[100:103], v201 offset:8192\n"
"s_waitcnt lgkmcnt(2)\n"
"v_mfma_f32_32x32x16_bf16 v[124:139], v[104:107], v[92:95], v[124:139]\n"
"ds_read_b64_tr_b16 v[120:121], v205 offset:4096\n"
"ds_read_b64_tr_b16 v[122:123], v205 offset:5120\n"
"s_waitcnt lgkmcnt(3)\n"
"v_mfma_f32_32x32x16_bf16 v[140:155], v[104:107], v[96:99], v[140:155]\n"
"ds_read_b128 v[108:111], v202 offset:0\n"
"ds_read_b128 v[112:115], v202 offset:4096\n"
"s_waitcnt lgkmcnt(4)\n"
"v_mfma_f32_32x32x16_bf16 v[156:171], v[104:107], v[100:103], v[156:171]\n"
"ds_read_b128 v[116:119], v202 offset:8192\n"
"s_waitcnt lgkmcnt(2)\n"
"v_mfma_f32_32x32x16_bf16 v[124:139], v[120:123], v[108:111], v[124:139]\n"
"ds_read_b64_tr_b16 v[104:105], v205 offset:8192\n"
"ds_read_b64_tr_b16 v[106:107], v205 offset:9216\n"
"s_waitcnt lgkmcnt(3)\n"
"v_mfma_f32_32x32x16_bf16 v[140:155], v[120:123], v[112:115], v[140:155]\n"
"ds_read_b128 v[92:95], v203 offset:0\n"
"ds_read_b128 v[96:99], v203 offset:4096\n"
"s_waitcnt lgkmcnt(4)\n"
"v_mfma_f32_32x32x16_bf16 v[156:171], v[120:123], v[116:119], v[156:171]\n"
"ds_read_b128 v[100:103], v203 offset:8192\n"
"s_waitcnt lgkmcnt(2)\n"
"v_mfma_f32_32x32x16_bf16 v[124:139], v[104:107], v[92:95], v[124:139]\n"
"ds_read_b64_tr_b16 v[120:121], v205 offset:12288\n"
"ds_read_b64_tr_b16 v[122:123], v205 offset:13312\n"
"s_waitcnt lgkmcnt(3)\n"
"v_mfma_f32_32x32x16_bf16 v[140:155], v[104:107], v[96:99], v[140:155]\n"
"ds_read_b128 v[108:111], v204 offset:0\n"
"ds_read_b128 v[112:115], v204 offset:4096\n"
"s_waitcnt lgkmcnt(4)\n"
"v_mfma_f32_32x32x16_bf16 v[156:171], v[104:107], v[100:103], v[156:171]\n"
"ds_read_b128 v[116:119], v204 offset:8192\n"
"s_waitcnt lgkmcnt(0)\n"
"v_mfma_f32_32x32x16_bf16 v[124:139], v[120:123], v[108:111], v[124:139]\n"
"s_barrier\n"
"ds_read_b64_tr_b16 v[104:105], v205 offset:28672\n"
"ds_read_b64_tr_b16 v[106:107], v205 offset:29696\n"
"v_mfma_f32_32x32x16_bf16 v[140:155], v[120:123], v[112:115], v[140:155]\n"
"ds_read_b128 v[92:95], v201 offset:28672\n"
"ds_read_b128 v[96:99], v201 offset:32768\n"
"v_mfma_f32_32x32x16_bf16 v[156:171], v[120:123], v[116:119], v[156:171]\n"
"ds_read_b128 v[100:103], v201 offset:36864\n"
"s_waitcnt lgkmcnt(2)\n"
"v_mfma_f32_32x32x16_bf16 v[124:139], v[104:107], v[92:95], v[124:139]\n"
"ds_read_b64_tr_b16 v[120:121], v205 offset:32768\n"
"ds_read_b64_tr_b16 v[122:123], v205 offset:33792\n"
"s_waitcnt lgkmcnt(3)\n"
"v_mfma_f32_32x32x16_bf16 v[140:155], v[104:107], v[96:99], v[140:155]\n"
"ds_read_b128 v[108:111], v202 offset:28672\n"
"ds_read_b128 v[112:115], v202 offset:32768\n"
"s_waitcnt lgkmcnt(4)\n"
"v_mfma_f32_32x32x16_bf16 v[156:171], v[104:107], v[100:103], v[156:171]\n"
"ds_read_b128 v[116:119], v202 offset:36864\n"
"s_waitcnt lgkmcnt(2)\n"
"v_mfma_f32_32x32x16_bf16 v[124:139], v[120:123], v[108:111], v[124:139]\n"
"ds_read_b64_tr_b16 v[104:105], v205 offset:36864\n"
"ds_read_b64_tr_b16 v[106:107], v205 offset:37888\n"
"s_waitcnt lgkmcnt(3)\n"
"v_mfma_f32_32x32x16_bf16 v[140:155], v[120:123], v[112:115], v[140:155]\n"
"ds_read_b128 v[92:95], v203 offset:28672\n"
"ds_read_b128 v[96:99], v203 offset:32768\n"
"s_waitcnt lgkmcnt(4)\n"
"v_mfma_f32_32x32x16_bf16 v[156:171], v[120:123], v[116:119], v[156:171]\n"
"ds_read_b128 v[100:103], v203 offset:36864\n"
"s_waitcnt lgkmcnt(2)\n"
"v_mfma_f32_32x32x16_bf16 v[124:139], v[104:107], v[92:95], v[124:139]\n"
"ds_read_b64_tr_b16 v[120:121], v205 offset:40960\n"
"ds_read_b64_tr_b16 v[122:123], v205 offset:41984\n"
"s_waitcnt lgkmcnt(3)\n"
"v_mfma_f32_32x32x16_bf16 v[140:155], v[104:107], v[96:99], v[140:155]\n"
"ds_read_b128 v[108:111], v204 offset:28672\n"
"ds_read_b128 v[112:115], v204 offset:32768\n"
"s_waitcnt lgkmcnt(4)\n"
"v_mfma_f32_32x32x16_bf16 v[156:171], v[104:107], v[100:103], v[156:171]\n"
"ds_read_b128 v[116:119], v204 offset:36864\n"
"s_waitcnt lgkmcnt(0)\n"
"v_mfma_f32_32x32x16_bf16 v[124:139], v[120:123], v[108:111], v[124:139]\n"
"s_barrier\n"
"ds_read_b64_tr_b16 v[104:105], v200 offset:0\n"
"ds_read_b64_tr_b16 v[106:107], v200 offset:1024\n"
"v_mfma_f32_32x32x16_bf16 v[140:155], v[120:123], v[112:115], v[140:155]\n"
"ds_read_b128 v[92:95], v196 offset:0\n"
"ds_read_b128 v[96:99], v196 offset:4096\n"
"v_mfma_f32_32x32x16_bf16 v[156:171], v[120:123], v[116:119], v[156:171]\n"
"ds_read_b128 v[100:103], v196 offset:8192\n"
"s_waitcnt lgkmcnt(2)\n"
"v_mfma_f32_32x32x16_bf16 v[124:139], v[104:107], v[92:95], v[124:139]\n"
"ds_read_b64_tr_b16 v[120:121], v200 offset:4096\n"
"ds_read_b64_tr_b16 v[122:123], v200 offset:5120\n"
"s_waitcnt lgkmcnt(3)\n"
"v_mfma_f32_32x32x16_bf16 v[140:155], v[104:107], v[96:99], v[140:155]\n"
"ds_read_b128 v[108:111], v197 offset:0\n"
"ds_read_b128 v[112:115], v197 offset:4096\n"
"s_waitcnt lgkmcnt(4)\n"
"v_mfma_f32_32x32x16_bf16 v[156:171], v[104:107], v[100:103], v[156:171]\n"
"ds_read_b128 v[116:119], v197 offset:8192\n"
"s_waitcnt lgkmcnt(2)\n"
"v_mfma_f32_32x32x16_bf16 v[124:139], v[120:123], v[108:111], v[124:139]\n"
"ds_read_b64_tr_b16 v[104:105], v200 offset:8192\n"
"ds_read_b64_tr_b16 v[106:107], v200 offset:9216\n"
"s_waitcnt lgkmcnt(3)\n"
"v_mfma_f32_32x32x16_bf16 v[140:155], v[120:123], v[112:115], v[140:155]\n"
"ds_read_b128 v[92:95], v198 offset:0\n"
"ds_read_b128 v[96:99], v198 offset:4096\n"
"s_waitcnt lgkmcnt(4)\n"
"v_mfma_f32_32x32x16_bf16 v[156:171], v[120:123], v[116:119], v[156:171]\n"
"ds_read_b128 v[100:103], v198 offset:8192\n"
"s_waitcnt lgkmcnt(2)\n"
"v_mfma_f32_32x32x16_bf16 v[124:139], v[104:107], v[92:95], v[124:139]\n"
"ds_read_b64_tr_b16 v[120:121], v200 offset:12288\n"
"ds_read_b64_tr_b16 v[122:123], v200 offset:13312\n"
"s_waitcnt lgkmcnt(3)\n"
"v_mfma_f32_32x32x16_bf16 v[140:155], v[104:107], v[96:99], v[140:155]\n"
"ds_read_b128 v[108:111], v199 offset:0\n"
"ds_read_b128 v[112:115], v199 offset:4096\n"
"s_waitcnt lgkmcnt(4)\n"
"v_mfma_f32_32x32x16_bf16 v[156:171], v[104:107], v[100:103], v[156:171]\n"
"ds_read_b128 v[116:119], v199 offset:8192\n"
"s_waitcnt lgkmcnt(0)\n"
"v_mfma_f32_32x32x16_bf16 v[124:139], v[120:123], v[108:111], v[124:139]\n"
"v_mfma_f32_32x32x16_bf16 v[140:155], v[120:123], v[112:115], v[140:155]\n"
"v_mfma_f32_32x32x16_bf16 v[156:171], v[120:123], v[116:119], v[156:171]\n"
"s_add_u32 s50, s58, 0\n"
"s_add_u32 s51, s50, 28672\n"
"s_add_u32 s52, s50, 57344\n"
"s_cmp_eq_u32 s41, 21\n"
"s_cselect_b32 s51, 2130706432, s51\n"
"s_cselect_b32 s52, 2130706432, s52\n"
"s_nop 3\n"
"v_mul_f32 v124, s60, v124\n"
"v_mul_f32 v125, s60, v125\n"
"v_mul_f32 v126, s60, v126\n"
"v_mul_f32 v127, s60, v127\n"
"v_mul_f32 v128, s60, v128\n"
"v_mul_f32 v129, s60, v129\n"
"v_mul_f32 v130, s60, v130\n"
"v_mul_f32 v131, s60, v131\n"
"v_mul_f32 v132, s60, v132\n"
"v_mul_f32 v133, s60, v133\n"
"v_mul_f32 v134, s60, v134\n"
"v_mul_f32 v135, s60, v135\n"
"v_mul_f32 v136, s60, v136\n"
"v_mul_f32 v137, s60, v137\n"
"v_mul_f32 v138, s60, v138\n"
"v_mul_f32 v139, s60, v139\n"
"v_cvt_pk_bf16_f32 v220, v124, v125\n"
"v_cvt_pk_bf16_f32 v221, v126, v127\n"
"v_cvt_pk_bf16_f32 v222, v128, v129\n"
"v_cvt_pk_bf16_f32 v223, v130, v131\n"
"v_cvt_pk_bf16_f32 v224, v132, v133\n"
"v_cvt_pk_bf16_f32 v225, v134, v135\n"
"v_cvt_pk_bf16_f32 v226, v136, v137\n"
"v_cvt_pk_bf16_f32 v227, v138, v139\n"
"v_permlane32_swap_b32 v220, v222\n"
"v_permlane32_swap_b32 v221, v223\n"
"v_permlane32_swap_b32 v224, v226\n"
"v_permlane32_swap_b32 v225, v227\n"
"buffer_store_dwordx4 v[220:223], v206, s[20:23], s50 offen sc1\n"
"buffer_store_dwordx4 v[224:227], v207, s[20:23], s50 offen sc1\n"
"v_mul_f32 v140, s60, v140\n"
"v_mul_f32 v141, s60, v141\n"
"v_mul_f32 v142, s60, v142\n"
"v_mul_f32 v143, s60, v143\n"
"v_mul_f32 v144, s60, v144\n"
"v_mul_f32 v145, s60, v145\n"
"v_mul_f32 v146, s60, v146\n"
"v_mul_f32 v147, s60, v147\n"
"v_mul_f32 v148, s60, v148\n"
"v_mul_f32 v149, s60, v149\n"
"v_mul_f32 v150, s60, v150\n"
"v_mul_f32 v151, s60, v151\n"
"v_mul_f32 v152, s60, v152\n"
"v_mul_f32 v153, s60, v153\n"
"v_mul_f32 v154, s60, v154\n"
"v_mul_f32 v155, s60, v155\n"
"v_cvt_pk_bf16_f32 v228, v140, v141\n"
"v_cvt_pk_bf16_f32 v229, v142, v143\n"
"v_cvt_pk_bf16_f32 v230, v144, v145\n"
"v_cvt_pk_bf16_f32 v231, v146, v147\n"
"v_cvt_pk_bf16_f32 v232, v148, v149\n"
"v_cvt_pk_bf16_f32 v233, v150, v151\n"
"v_cvt_pk_bf16_f32 v234, v152, v153\n"
"v_cvt_pk_bf16_f32 v235, v154, v155\n"
"v_permlane32_swap_b32 v228, v230\n"
"v_permlane32_swap_b32 v229, v231\n"
"v_permlane32_swap_b32 v232, v234\n"
"v_permlane32_swap_b32 v233, v235\n"
"buffer_store_dwordx4 v[228:231], v206, s[20:23], s51 offen sc1\n"
"buffer_store_dwordx4 v[232:235], v207, s[20:23], s51 offen sc1\n"
"v_mul_f32 v156, s60, v156\n"
"v_mul_f32 v157, s60, v157\n"
"v_mul_f32 v158, s60, v158\n"
"v_mul_f32 v159, s60, v159\n"
"v_mul_f32 v160, s60, v160\n"
"v_mul_f32 v161, s60, v161\n"
"v_mul_f32 v162, s60, v162\n"
"v_mul_f32 v163, s60, v163\n"
"v_mul_f32 v164, s60, v164\n"
"v_mul_f32 v165, s60, v165\n"
"v_mul_f32 v166, s60, v166\n"
"v_mul_f32 v167, s60, v167\n"
"v_mul_f32 v168, s60, v168\n"
"v_mul_f32 v169, s60, v169\n"
"v_mul_f32 v170, s60, v170\n"
"v_mul_f32 v171, s60, v171\n"
"v_cvt_pk_bf16_f32 v236, v156, v157\n"
"v_cvt_pk_bf16_f32 v237, v158, v159\n"
"v_cvt_pk_bf16_f32 v238, v160, v161\n"
"v_cvt_pk_bf16_f32 v239, v162, v163\n"
"v_cvt_pk_bf16_f32 v240, v164, v165\n"
"v_cvt_pk_bf16_f32 v241, v166, v167\n"
"v_cvt_pk_bf16_f32 v242, v168, v169\n"
"v_cvt_pk_bf16_f32 v243, v170, v171\n"
"v_permlane32_swap_b32 v236, v238\n"
"v_permlane32_swap_b32 v237, v239\n"
"v_permlane32_swap_b32 v240, v242\n"
"v_permlane32_swap_b32 v241, v243\n"
"buffer_store_dwordx4 v[236:239], v206, s[20:23], s52 offen sc1\n"
"buffer_store_dwordx4 v[240:243], v207, s[20:23], s52 offen sc1\n"
"s_barrier\n"
"ds_read_b64_tr_b16 v[104:105], v200 offset:28672\n"
"ds_read_b64_tr_b16 v[106:107], v200 offset:29696\n"
"ds_read_b128 v[92:95], v196 offset:28672\n"
"ds_read_b128 v[96:99], v196 offset:32768\n"
"ds_read_b128 v[100:103], v196 offset:36864\n"
"s_waitcnt lgkmcnt(2)\n"
"v_mfma_f32_32x32x16_bf16 v[124:139], v[104:107], v[92:95], 0\n"
"ds_read_b64_tr_b16 v[120:121], v200 offset:32768\n"
"ds_read_b64_tr_b16 v[122:123], v200 offset:33792\n"
"s_waitcnt lgkmcnt(3)\n"
"v_mfma_f32_32x32x16_bf16 v[140:155], v[104:107], v[96:99], 0\n"
"ds_read_b128 v[108:111], v197 offset:28672\n"
"ds_read_b128 v[112:115], v197 offset:32768\n"
"s_waitcnt lgkmcnt(4)\n"
"v_mfma_f32_32x32x16_bf16 v[156:171], v[104:107], v[100:103], 0\n"
"ds_read_b128 v[116:119], v197 offset:36864\n"
"s_waitcnt lgkmcnt(2)\n"
"v_mfma_f32_32x32x16_bf16 v[124:139], v[120:123], v[108:111], v[124:139]\n"
"ds_read_b64_tr_b16 v[104:105], v200 offset:36864\n"
"ds_read_b64_tr_b16 v[106:107], v200 offset:37888\n"
"s_waitcnt lgkmcnt(3)\n"
"v_mfma_f32_32x32x16_bf16 v[140:155], v[120:123], v[112:115], v[140:155]\n"
"ds_read_b128 v[92:95], v198 offset:28672\n"
"ds_read_b128 v[96:99], v198 offset:32768\n"
"s_waitcnt lgkmcnt(4)\n"
"v_mfma_f32_32x32x16_bf16 v[156:171], v[120:123], v[116:119], v[156:171]\n"
"ds_read_b128 v[100:103], v198 offset:36864\n"
"s_waitcnt lgkmcnt(2)\n"
"v_mfma_f32_32x32x16_bf16 v[124:139], v[104:107], v[92:95], v[124:139]\n"
"ds_read_b64_tr_b16 v[120:121], v200 offset:40960\n"
"ds_read_b64_tr_b16 v[122:123], v200 offset:41984\n"
"s_waitcnt lgkmcnt(3)\n"
"v_mfma_f32_32x32x16_bf16 v[140:155], v[104:107], v[96:99], v[140:155]\n"
"ds_read_b128 v[108:111], v199 offset:28672\n"
"ds_read_b128 v[112:115], v199 offset:32768\n"
"s_waitcnt lgkmcnt(4)\n"
"v_mfma_f32_32x32x16_bf16 v[156:171], v[104:107], v[100:103], v[156:171]\n"
"ds_read_b128 v[116:119], v199 offset:36864\n"
"s_waitcnt lgkmcnt(0)\n"
"v_mfma_f32_32x32x16_bf16 v[124:139], v[120:123], v[108:111], v[124:139]\n"
"s_barrier\n"
"ds_read_b64_tr_b16 v[104:105], v205 offset:0\n"
"ds_read_b64_tr_b16 v[106:107], v205 offset:1024\n"
"v_mfma_f32_32x32x16_bf16 v[140:155], v[120:123], v[112:115], v[140:155]\n"
"ds_read_b128 v[92:95], v201 offset:0\n"
"ds_read_b128 v[96:99], v201 offset:4096\n"
"v_mfma_f32_32x32x16_bf16 v[156:171], v[120:123], v[116:119], v[156:171]\n"
"ds_read_b128 v[100:103], v201 offset:8192\n"
"s_waitcnt lgkmcnt(2)\n"
"v_mfma_f32_32x32x16_bf16 v[124:139], v[104:107], v[92:95], v[124:139]\n"
"ds_read_b64_tr_b16 v[120:121], v205 offset:4096\n"
"ds_read_b64_tr_b16 v[122:123], v205 offset:5120\n"
"s_waitcnt lgkmcnt(3)\n"
"v_mfma_f32_32x32x16_bf16 v[140:155], v[104:107], v[96:99], v[140:155]\n"
"ds_read_b128 v[108:111], v202 offset:0\n"
"ds_read_b128 v[112:115], v202 offset:4096\n"
"s_waitcnt lgkmcnt(4)\n"
"v_mfma_f32_32x32x16_bf16 v[156:171], v[104:107], v[100:103], v[156:171]\n"
"ds_read_b128 v[116:119], v202 offset:8192\n"
"s_waitcnt lgkmcnt(2)\n"
"v_mfma_f32_32x32x16_bf16 v[124:139], v[120:123], v[108:111], v[124:139]\n"
"ds_read_b64_tr_b16 v[104:105], v205 offset:8192\n"
"ds_read_b64_tr_b16 v[106:107], v205 offset:9216\n"
"s_waitcnt lgkmcnt(3)\n"
"v_mfma_f32_32x32x16_bf16 v[140:155], v[120:123], v[112:115], v[140:155]\n"
"ds_read_b128 v[92:95], v203 offset:0\n"
"ds_read_b128 v[96:99], v203 offset:4096\n"
"s_waitcnt lgkmcnt(4)\n"
"v_mfma_f32_32x32x16_bf16 v[156:171], v[120:123], v[116:119], v[156:171]\n"
"ds_read_b128 v[100:103], v203 offset:8192\n"
"s_waitcnt lgkmcnt(2)\n"
"v_mfma_f32_32x32x16_bf16 v[124:139], v[104:107], v[92:95], v[124:139]\n"
"ds_read_b64_tr_b16 v[120:121], v205 offset:12288\n"
"ds_read_b64_tr_b16 v[122:123], v205 offset:13312\n"
"s_waitcnt lgkmcnt(3)\n"
"v_mfma_f32_32x32x16_bf16 v[140:155], v[104:107], v[96:99], v[140:155]\n"
"ds_read_b128 v[108:111], v204 offset:0\n"
"ds_read_b128 v[112:115], v204 offset:4096\n"
"s_waitcnt lgkmcnt(4)\n"
"v_mfma_f32_32x32x16_bf16 v[156:171], v[104:107], v[100:103], v[156:171]\n"
"ds_read_b128 v[116:119], v204 offset:8192\n"
"s_waitcnt lgkmcnt(0)\n"
"v_mfma_f32_32x32x16_bf16 v[124:139], v[120:123], v[108:111], v[124:139]\n"
"s_waitcnt vmcnt(0)\n"
"s_barrier\n"
"s_barrier\n"
"ds_read_b64_tr_b16 v[104:105], v205 offset:28672\n"
"ds_read_b64_tr_b16 v[106:107], v205 offset:29696\n"
"v_mfma_f32_32x32x16_bf16 v[140:155], v[120:123], v[112:115], v[140:155]\n"
"ds_read_b128 v[92:95], v201 offset:28672\n"
"ds_read_b128 v[96:99], v201 offset:32768\n"
"v_mfma_f32_32x32x16_bf16 v[156:171], v[120:123], v[116:119], v[156:171]\n"
"ds_read_b128 v[100:103], v201 offset:36864\n"
"s_waitcnt lgkmcnt(2)\n"
"v_mfma_f32_32x32x16_bf16 v[124:139], v[104:107], v[92:95], v[124:139]\n"
"ds_read_b64_tr_b16 v[120:121], v205 offset:32768\n"
"ds_read_b64_tr_b16 v[122:123], v205 offset:33792\n"
"s_waitcnt lgkmcnt(3)\n"
"v_mfma_f32_32x32x16_bf16 v[140:155], v[104:107], v[96:99], v[140:155]\n"
"ds_read_b128 v[108:111], v202 offset:28672\n"
"ds_read_b128 v[112:115], v202 offset:32768\n"
"s_waitcnt lgkmcnt(4)\n"
"v_mfma_f32_32x32x16_bf16 v[156:171], v[104:107], v[100:103], v[156:171]\n"
"ds_read_b128 v[116:119], v202 offset:36864\n"
"s_waitcnt lgkmcnt(2)\n"
"v_mfma_f32_32x32x16_bf16 v[124:139], v[120:123], v[108:111], v[124:139]\n"
"ds_read_b64_tr_b16 v[104:105], v205 offset:36864\n"
"ds_read_b64_tr_b16 v[106:107], v205 offset:37888\n"
"s_waitcnt lgkmcnt(3)\n"
"v_mfma_f32_32x32x16_bf16 v[140:155], v[120:123], v[112:115], v[140:155]\n"
"ds_read_b128 v[92:95], v203 offset:28672\n"
"ds_read_b128 v[96:99], v203 offset:32768\n"
"s_waitcnt lgkmcnt(4)\n"
"v_mfma_f32_32x32x16_bf16 v[156:171], v[120:123], v[116:119], v[156:171]\n"
"ds_read_b128 v[100:103], v203 offset:36864\n"
"s_waitcnt lgkmcnt(2)\n"
"v_mfma_f32_32x32x16_bf16 v[124:139], v[104:107], v[92:95], v[124:139]\n"
"ds_read_b64_tr_b16 v[120:121], v205 offset:40960\n"
"ds_read_b64_tr_b16 v[122:123], v205 offset:41984\n"
"s_waitcnt lgkmcnt(3)\n"
"v_mfma_f32_32x32x16_bf16 v[140:155], v[104:107], v[96:99], v[140:155]\n"
"ds_read_b128 v[108:111], v204 offset:28672\n"
"ds_read_b128 v[112:115], v204 offset:32768\n"
"s_waitcnt lgkmcnt(4)\n"
"v_mfma_f32_32x32x16_bf16 v[156:171], v[104:107], v[100:103], v[156:171]\n"
"ds_read_b128 v[116:119], v204 offset:36864\n"
"s_waitcnt lgkmcnt(0)\n"
"v_mfma_f32_32x32x16_bf16 v[124:139], v[120:123], v[108:111], v[124:139]\n"
"s_barrier\n"
"ds_read_b64_tr_b16 v[104:105], v200 offset:0\n"
"ds_read_b64_tr_b16 v[106:107], v200 offset:1024\n"
"v_mfma_f32_32x32x16_bf16 v[140:155], v[120:123], v[112:115], v[140:155]\n"
"ds_read_b128 v[92:95], v196 offset:0\n"
"ds_read_b128 v[96:99], v196 offset:4096\n"
"v_mfma_f32_32x32x16_bf16 v[156:171], v[120:123], v[116:119], v[156:171]\n"
"ds_read_b128 v[100:103], v196 offset:8192\n"
"s_waitcnt lgkmcnt(2)\n"
"v_mfma_f32_32x32x16_bf16 v[124:139], v[104:107], v[92:95], v[124:139]\n"
"ds_read_b64_tr_b16 v[120:121], v200 offset:4096\n"
"ds_read_b64_tr_b16 v[122:123], v200 offset:5120\n"
"s_waitcnt lgkmcnt(3)\n"
"v_mfma_f32_32x32x16_bf16 v[140:155], v[104:107], v[96:99], v[140:155]\n"
"ds_read_b128 v[108:111], v197 offset:0\n"
"ds_read_b128 v[112:115], v197 offset:4096\n"
"s_waitcnt lgkmcnt(4)\n"
"v_mfma_f32_32x32x16_bf16 v[156:171], v[104:107], v[100:103], v[156:171]\n"
"ds_read_b128 v[116:119], v197 offset:8192\n"
"s_waitcnt lgkmcnt(2)\n"
"v_mfma_f32_32x32x16_bf16 v[124:139], v[120:123], v[108:111], v[124:139]\n"
"ds_read_b64_tr_b16 v[104:105], v200 offset:8192\n"
"ds_read_b64_tr_b16 v[106:107], v200 offset:9216\n"
"s_waitcnt lgkmcnt(3)\n"
"v_mfma_f32_32x32x16_bf16 v[140:155], v[120:123], v[112:115], v[140:155]\n"
"ds_read_b128 v[92:95], v198 offset:0\n"
"ds_read_b128 v[96:99], v198 offset:4096\n"
"s_waitcnt lgkmcnt(4)\n"
"v_mfma_f32_32x32x16_bf16 v[156:171], v[120:123], v[116:119], v[156:171]\n"
"ds_read_b128 v[100:103], v198 offset:8192\n"
"s_waitcnt lgkmcnt(2)\n"
"v_mfma_f32_32x32x16_bf16 v[124:139], v[104:107], v[92:95], v[124:139]\n"
"ds_read_b64_tr_b16 v[120:121], v200 offset:12288\n"
"ds_read_b64_tr_b16 v[122:123], v200 offset:13312\n"
"s_waitcnt lgkmcnt(3)\n"
"v_mfma_f32_32x32x16_bf16 v[140:155], v[104:107], v[96:99], v[140:155]\n"
"ds_read_b128 v[108:111], v199 offset:0\n"
"ds_read_b128 v[112:115], v199 offset:4096\n"
"s_waitcnt lgkmcnt(4)\n"
"v_mfma_f32_32x32x16_bf16 v[156:171], v[104:107], v[100:103], v[156:171]\n"
"ds_read_b128 v[116:119], v199 offset:8192\n"
"s_waitcnt lgkmcnt(0)\n"
"v_mfma_f32_32x32x16_bf16 v[124:139], v[120:123], v[108:111], v[124:139]\n"
"s_barrier\n"
"ds_read_b64_tr_b16 v[104:105], v200 offset:28672\n"
"ds_read_b64_tr_b16 v[106:107], v200 offset:29696\n"
"v_mfma_f32_32x32x16_bf16 v[140:155], v[120:123], v[112:115], v[140:155]\n"
"ds_read_b128 v[92:95], v196 offset:28672\n"
"ds_read_b128 v[96:99], v196 offset:32768\n"
"v_mfma_f32_32x32x16_bf16 v[156:171], v[120:123], v[116:119], v[156:171]\n"
"ds_read_b128 v[100:103], v196 offset:36864\n"
"s_waitcnt lgkmcnt(2)\n"
"v_mfma_f32_32x32x16_bf16 v[124:139], v[104:107], v[92:95], v[124:139]\n"
"ds_read_b64_tr_b16 v[120:121], v200 offset:32768\n"
"ds_read_b64_tr_b16 v[122:123], v200 offset:33792\n"
"s_waitcnt lgkmcnt(3)\n"
"v_mfma_f32_32x32x16_bf16 v[140:155], v[104:107], v[96:99], v[140:155]\n"
"ds_read_b128 v[108:111], v197 offset:28672\n"
"ds_read_b128 v[112:115], v197 offset:32768\n"
"s_waitcnt lgkmcnt(4)\n"
"v_mfma_f32_32x32x16_bf16 v[156:171], v[104:107], v[100:103], v[156:171]\n"
"ds_read_b128 v[116:119], v197 offset:36864\n"
"s_waitcnt lgkmcnt(2)\n"
"v_mfma_f32_32x32x16_bf16 v[124:139], v[120:123], v[108:111], v[124:139]\n"
"ds_read_b64_tr_b16 v[104:105], v200 offset:36864\n"
"ds_read_b64_tr_b16 v[106:107], v200 offset:37888\n"
"s_waitcnt lgkmcnt(3)\n"
"v_mfma_f32_32x32x16_bf16 v[140:155], v[120:123], v[112:115], v[140:155]\n"
"ds_read_b128 v[92:95], v198 offset:28672\n"
"ds_read_b128 v[96:99], v198 offset:32768\n"
"s_waitcnt lgkmcnt(4)\n"
"v_mfma_f32_32x32x16_bf16 v[156:171], v[120:123], v[116:119], v[156:171]\n"
"ds_read_b128 v[100:103], v198 offset:36864\n"
"s_waitcnt lgkmcnt(2)\n"
"v_mfma_f32_32x32x16_bf16 v[124:139], v[104:107], v[92:95], v[124:139]\n"
"ds_read_b64_tr_b16 v[120:121], v200 offset:40960\n"
"ds_read_b64_tr_b16 v[122:123], v200 offset:41984\n"
"s_waitcnt lgkmcnt(3)\n"
"v_mfma_f32_32x32x16_bf16 v[140:155], v[104:107], v[96:99], v[140:155]\n"
"ds_read_b128 v[108:111], v199 offset:28672\n"
"ds_read_b128 v[112:115], v199 offset:32768\n"
"s_waitcnt lgkmcnt(4)\n"
"v_mfma_f32_32x32x16_bf16 v[156:171], v[104:107], v[100:103], v[156:171]\n"
"ds_read_b128 v[116:119], v199 offset:36864\n"
"s_waitcnt lgkmcnt(0)\n"
"v_mfma_f32_32x32x16_bf16 v[124:139], v[120:123], v[108:111], v[124:139]\n"
"s_barrier\n"
"ds_read_b64_tr_b16 v[104:105], v205 offset:0\n"
"ds_read_b64_tr_b16 v[106:107], v205 offset:1024\n"
"v_mfma_f32_32x32x16_bf16 v[140:155], v[120:123], v[112:115], v[140:155]\n"
"ds_read_b128 v[92:95], v201 offset:0\n"
"ds_read_b128 v[96:99], v201 offset:4096\n"
"v_mfma_f32_32x32x16_bf16 v[156:171], v[120:123], v[116:119], v[156:171]\n"
"ds_read_b128 v[100:103], v201 offset:8192\n"
"s_waitcnt lgkmcnt(2)\n"
"v_mfma_f32_32x32x16_bf16 v[124:139], v[104:107], v[92:95], v[124:139]\n"
"ds_read_b64_tr_b16 v[120:121], v205 offset:4096\n"
"ds_read_b64_tr_b16 v[122:123], v205 offset:5120\n"
"s_waitcnt lgkmcnt(3)\n"
"v_mfma_f32_32x32x16_bf16 v[140:155], v[104:107], v[96:99], v[140:155]\n"
"ds_read_b128 v[108:111], v202 offset:0\n"
"ds_read_b128 v[112:115], v202 offset:4096\n"
"s_waitcnt lgkmcnt(4)\n"
"v_mfma_f32_32x32x16_bf16 v[156:171], v[104:107], v[100:103], v[156:171]\n"
"ds_read_b128 v[116:119], v202 offset:8192\n"
"s_waitcnt lgkmcnt(2)\n"
"v_mfma_f32_32x32x16_bf16 v[124:139], v[120:123], v[108:111], v[124:139]\n"
"ds_read_b64_tr_b16 v[104:105], v205 offset:8192\n"
"ds_read_b64_tr_b16 v[106:107], v205 offset:9216\n"
"s_waitcnt lgkmcnt(3)\n"
"v_mfma_f32_32x32x16_bf16 v[140:155], v[120:123], v[112:115], v[140:155]\n"
"ds_read_b128 v[92:95], v203 offset:0\n"
"ds_read_b128 v[96:99], v203 offset:4096\n"
"s_waitcnt lgkmcnt(4)\n"
"v_mfma_f32_32x32x16_bf16 v[156:171], v[120:123], v[116:119], v[156:171]\n"
"ds_read_b128 v[100:103], v203 offset:8192\n"
"s_waitcnt lgkmcnt(2)\n"
"v_mfma_f32_32x32x16_bf16 v[124:139], v[104:107], v[92:95], v[124:139]\n"
"ds_read_b64_tr_b16 v[120:121], v205 offset:12288\n"
"ds_read_b64_tr_b16 v[122:123], v205 offset:13312\n"
"s_waitcnt lgkmcnt(3)\n"
"v_mfma_f32_32x32x16_bf16 v[140:155], v[104:107], v[96:99], v[140:155]\n"
"ds_read_b128 v[108:111], v204 offset:0\n"
"ds_read_b128 v[112:115], v204 offset:4096\n"
"s_waitcnt lgkmcnt(4)\n"
"v_mfma_f32_32x32x16_bf16 v[156:171], v[104:107], v[100:103], v[156:171]\n"
"ds_read_b128 v[116:119], v204 offset:8192\n"
"s_waitcnt lgkmcnt(0)\n"
"v_mfma_f32_32x32x16_bf16 v[124:139], v[120:123], v[108:111], v[124:139]\n"
"s_barrier\n"
"ds_read_b64_tr_b16 v[104:105], v205 offset:28672\n"
"ds_read_b64_tr_b16 v[106:107], v205 offset:29696\n"
"v_mfma_f32_32x32x16_bf16 v[140:155], v[120:123], v[112:115], v[140:155]\n"
"ds_read_b128 v[92:95], v201 offset:28672\n"
"ds_read_b128 v[96:99], v201 offset:32768\n"
"v_mfma_f32_32x32x16_bf16 v[156:171], v[120:123], v[116:119], v[156:171]\n"
"ds_read_b128 v[100:103], v201 offset:36864\n"
"s_waitcnt lgkmcnt(2)\n"
"v_mfma_f32_32x32x16_bf16 v[124:139], v[104:107], v[92:95], v[124:139]\n"
"ds_read_b64_tr_b16 v[120:121], v205 offset:32768\n"
"ds_read_b64_tr_b16 v[122:123], v205 offset:33792\n"
"s_waitcnt lgkmcnt(3)\n"
"v_mfma_f32_32x32x16_bf16 v[140:155], v[104:107], v[96:99], v[140:155]\n"
"ds_read_b128 v[108:111], v202 offset:28672\n"
"ds_read_b128 v[112:115], v202 offset:32768\n"
"s_waitcnt lgkmcnt(4)\n"
"v_mfma_f32_32x32x16_bf16 v[156:171], v[104:107], v[100:103], v[156:171]\n"
"ds_read_b128 v[116:119], v202 offset:36864\n"
"s_waitcnt lgkmcnt(2)\n"
"v_mfma_f32_32x32x16_bf16 v[124:139], v[120:123], v[108:111], v[124:139]\n"
"ds_read_b64_tr_b16 v[104:105], v205 offset:36864\n"
"ds_read_b64_tr_b16 v[106:107], v205 offset:37888\n"
"s_waitcnt lgkmcnt(3)\n"
"v_mfma_f32_32x32x16_bf16 v[140:155], v[120:123], v[112:115], v[140:155]\n"
"ds_read_b128 v[92:95], v203 offset:28672\n"
"ds_read_b128 v[96:99], v203 offset:32768\n"
"s_waitcnt lgkmcnt(4)\n"
"v_mfma_f32_32x32x16_bf16 v[156:171], v[120:123], v[116:119], v[156:171]\n"
"ds_read_b128 v[100:103], v203 offset:36864\n"
"s_waitcnt lgkmcnt(2)\n"
"v_mfma_f32_32x32x16_bf16 v[124:139], v[104:107], v[92:95], v[124:139]\n"
"ds_read_b64_tr_b16 v[120:121], v205 offset:40960\n"
"ds_read_b64_tr_b16 v[122:123], v205 offset:41984\n"
"s_waitcnt lgkmcnt(3)\n"
"v_mfma_f32_32x32x16_bf16 v[140:155], v[104:107], v[96:99], v[140:155]\n"
"ds_read_b128 v[108:111], v204 offset:28672\n"
"ds_read_b128 v[112:115], v204 offset:32768\n"
"s_waitcnt lgkmcnt(4)\n"
"v_mfma_f32_32x32x16_bf16 v[156:171], v[104:107], v[100:103], v[156:171]\n"
"ds_read_b128 v[116:119], v204 offset:36864\n"
"s_waitcnt lgkmcnt(0)\n"
"v_mfma_f32_32x32x16_bf16 v[124:139], v[120:123], v[108:111], v[124:139]\n"
"v_mfma_f32_32x32x16_bf16 v[140:155], v[120:123], v[112:115], v[140:155]\n"
"v_mfma_f32_32x32x16_bf16 v[156:171], v[120:123], v[116:119], v[156:171]\n"
"s_nop 9\n"
"v_cvt_pk_bf16_f32 v220, v124, v125\n"
"v_cvt_pk_bf16_f32 v221, v126, v127\n"
"v_cvt_pk_bf16_f32 v222, v128, v129\n"
"v_cvt_pk_bf16_f32 v223, v130, v131\n"
"v_cvt_pk_bf16_f32 v224, v132, v133\n"
"v_cvt_pk_bf16_f32 v225, v134, v135\n"
"v_cvt_pk_bf16_f32 v226, v136, v137\n"
"v_cvt_pk_bf16_f32 v227, v138, v139\n"
"v_permlane32_swap_b32 v220, v222\n"
"v_permlane32_swap_b32 v221, v223\n"
"v_permlane32_swap_b32 v224, v226\n"
"v_permlane32_swap_b32 v225, v227\n"
"v_cvt_pk_bf16_f32 v228, v140, v141\n"
"v_cvt_pk_bf16_f32 v229, v142, v143\n"
"v_cvt_pk_bf16_f32 v230, v144, v145\n"
"v_cvt_pk_bf16_f32 v231, v146, v147\n"
"v_cvt_pk_bf16_f32 v232, v148, v149\n"
"v_cvt_pk_bf16_f32 v233, v150, v151\n"
"v_cvt_pk_bf16_f32 v234, v152, v153\n"
"v_cvt_pk_bf16_f32 v235, v154, v155\n"
"v_permlane32_swap_b32 v228, v230\n"
"v_permlane32_swap_b32 v229, v231\n"
"v_permlane32_swap_b32 v232, v234\n"
"v_permlane32_swap_b32 v233, v235\n"
"v_cvt_pk_bf16_f32 v236, v156, v157\n"
"v_cvt_pk_bf16_f32 v237, v158, v159\n"
"v_cvt_pk_bf16_f32 v238, v160, v161\n"
"v_cvt_pk_bf16_f32 v239, v162, v163\n"
"v_cvt_pk_bf16_f32 v240, v164, v165\n"
"v_cvt_pk_bf16_f32 v241, v166, v167\n"
"v_cvt_pk_bf16_f32 v242, v168, v169\n"
"v_cvt_pk_bf16_f32 v243, v170, v171\n"
"v_permlane32_swap_b32 v236, v238\n"
"v_permlane32_swap_b32 v237, v239\n"
"v_permlane32_swap_b32 v240, v242\n"
"v_permlane32_swap_b32 v241, v243\n"
"s_branch .Lfused_fwd_pro_c\n"
".Lfused_fwd_gcomp_v:\n"
"s_barrier\n"
"ds_read_b64_tr_b16 v[104:105], v205 offset:0\n"
"ds_read_b64_tr_b16 v[106:107], v205 offset:1024\n"
"ds_read_b128 v[92:95], v201 offset:0\n"
"ds_read_b128 v[96:99], v201 offset:4096\n"
"ds_read_b128 v[100:103], v201 offset:8192\n"
"s_waitcnt lgkmcnt(2)\n"
"v_mfma_f32_32x32x16_bf16 v[124:139], v[92:95], v[104:107], 0\n"
"ds_read_b64_tr_b16 v[120:121], v205 offset:4096\n"
"ds_read_b64_tr_b16 v[122:123], v205 offset:5120\n"
"s_waitcnt lgkmcnt(3)\n"
"v_mfma_f32_32x32x16_bf16 v[140:155], v[96:99], v[104:107], 0\n"
"ds_read_b128 v[108:111], v202 offset:0\n"
"ds_read_b128 v[112:115], v202 offset:4096\n"
"s_waitcnt lgkmcnt(4)\n"
"v_mfma_f32_32x32x16_bf16 v[156:171], v[100:103], v[104:107], 0\n"
"ds_read_b128 v[116:119], v202 offset:8192\n"
"s_waitcnt lgkmcnt(2)\n"
"v_mfma_f32_32x32x16_bf16 v[124:139], v[108:111], v[120:123], v[124:139]\n"
"ds_read_b64_tr_b16 v[104:105], v205 offset:8192\n"
"ds_read_b64_tr_b16 v[106:107], v205 offset:9216\n"
"s_waitcnt lgkmcnt(3)\n"
"v_mfma_f32_32x32x16_bf16 v[140:155], v[112:115], v[120:123], v[140:155]\n"
"ds_read_b128 v[92:95], v203 offset:0\n"
"ds_read_b128 v[96:99], v203 offset:4096\n"
"s_waitcnt lgkmcnt(4)\n"
"v_mfma_f32_32x32x16_bf16 v[156:171], v[116:119], v[120:123], v[156:171]\n"
"ds_read_b128 v[100:103], v203 offset:8192\n"
"s_waitcnt lgkmcnt(2)\n"
"v_mfma_f32_32x32x16_bf16 v[124:139], v[92:95], v[104:107], v[124:139]\n"
"ds_read_b64_tr_b16 v[120:121], v205 offset:12288\n"
"ds_read_b64_tr_b16 v[122:123], v205 offset:13312\n"
"s_waitcnt lgkmcnt(3)\n"
"v_mfma_f32_32x32x16_bf16 v[140:155], v[96:99], v[104:107], v[140:155]\n"
"ds_read_b128 v[108:111], v204 offset:0\n"
"ds_read_b128 v[112:115], v204 offset:4096\n"
"s_waitcnt lgkmcnt(4)\n"
"v_mfma_f32_32x32x16_bf16 v[156:171], v[100:103], v[104:107], v[156:171]\n"
"ds_read_b128 v[116:119], v204 offset:8192\n"
"s_waitcnt lgkmcnt(0)\n"
"v_mfma_f32_32x32x16_bf16 v[124:139], v[108:111], v[120:123], v[124:139]\n"
"s_barrier\n"
"ds_read_b64_tr_b16 v[104:105], v205 offset:28672\n"
"ds_read_b64_tr_b16 v[106:107], v205 offset:29696\n"
"v_mfma_f32_32x32x16_bf16 v[140:155], v[112:115], v[120:123], v[140:155]\n"
"ds_read_b128 v[92:95], v201 offset:28672\n"
"ds_read_b128 v[96:99], v201 offset:32768\n"
"v_mfma_f32_32x32x16_bf16 v[156:171], v[116:119], v[120:123], v[156:171]\n"
"ds_read_b128 v[100:103], v201 offset:36864\n"
"s_waitcnt lgkmcnt(2)\n"
"v_mfma_f32_32x32x16_bf16 v[124:139], v[92:95], v[104:107], v[124:139]\n"
"ds_read_b64_tr_b16 v[120:121], v205 offset:32768\n"
"ds_read_b64_tr_b16 v[122:123], v205 offset:33792\n"
"s_waitcnt lgkmcnt(3)\n"
"v_mfma_f32_32x32x16_bf16 v[140:155], v[96:99], v[104:107], v[140:155]\n"
"ds_read_b128 v[108:111], v202 offset:28672\n"
"ds_read_b128 v[112:115], v202 offset:32768\n"
"s_waitcnt lgkmcnt(4)\n"
"v_mfma_f32_32x32x16_bf16 v[156:171], v[100:103], v[104:107], v[156:171]\n"
"ds_read_b128 v[116:119], v202 offset:36864\n"
"s_waitcnt lgkmcnt(2)\n"
"v_mfma_f32_32x32x16_bf16 v[124:139], v[108:111], v[120:123], v[124:139]\n"
"ds_read_b64_tr_b16 v[104:105], v205 offset:36864\n"
"ds_read_b64_tr_b16 v[106:107], v205 offset:37888\n"
"s_waitcnt lgkmcnt(3)\n"
"v_mfma_f32_32x32x16_bf16 v[140:155], v[112:115], v[120:123], v[140:155]\n"
"ds_read_b128 v[92:95], v203 offset:28672\n"
"ds_read_b128 v[96:99], v203 offset:32768\n"
"s_waitcnt lgkmcnt(4)\n"
"v_mfma_f32_32x32x16_bf16 v[156:171], v[116:119], v[120:123], v[156:171]\n"
"ds_read_b128 v[100:103], v203 offset:36864\n"
"s_waitcnt lgkmcnt(2)\n"
"v_mfma_f32_32x32x16_bf16 v[124:139], v[92:95], v[104:107], v[124:139]\n"
"ds_read_b64_tr_b16 v[120:121], v205 offset:40960\n"
"ds_read_b64_tr_b16 v[122:123], v205 offset:41984\n"
"s_waitcnt lgkmcnt(3)\n"
"v_mfma_f32_32x32x16_bf16 v[140:155], v[96:99], v[104:107], v[140:155]\n"
"ds_read_b128 v[108:111], v204 offset:28672\n"
"ds_read_b128 v[112:115], v204 offset:32768\n"
"s_waitcnt lgkmcnt(4)\n"
"v_mfma_f32_32x32x16_bf16 v[156:171], v[100:103], v[104:107], v[156:171]\n"
"ds_read_b128 v[116:119], v204 offset:36864\n"
"s_waitcnt lgkmcnt(0)\n"
"v_mfma_f32_32x32x16_bf16 v[124:139], v[108:111], v[120:123], v[124:139]\n"
"s_barrier\n"
"ds_read_b64_tr_b16 v[104:105], v200 offset:0\n"
"ds_read_b64_tr_b16 v[106:107], v200 offset:1024\n"
"v_mfma_f32_32x32x16_bf16 v[140:155], v[112:115], v[120:123], v[140:155]\n"
"ds_read_b128 v[92:95], v196 offset:0\n"
"ds_read_b128 v[96:99], v196 offset:4096\n"
"v_mfma_f32_32x32x16_bf16 v[156:171], v[116:119], v[120:123], v[156:171]\n"
"ds_read_b128 v[100:103], v196 offset:8192\n"
"s_waitcnt lgkmcnt(2)\n"
"v_mfma_f32_32x32x16_bf16 v[124:139], v[92:95], v[104:107], v[124:139]\n"
"ds_read_b64_tr_b16 v[120:121], v200 offset:4096\n"
"ds_read_b64_tr_b16 v[122:123], v200 offset:5120\n"
"s_waitcnt lgkmcnt(3)\n"
"v_mfma_f32_32x32x16_bf16 v[140:155], v[96:99], v[104:107], v[140:155]\n"
"ds_read_b128 v[108:111], v197 offset:0\n"
"ds_read_b128 v[112:115], v197 offset:4096\n"
"s_waitcnt lgkmcnt(4)\n"
"v_mfma_f32_32x32x16_bf16 v[156:171], v[100:103], v[104:107], v[156:171]\n"
"ds_read_b128 v[116:119], v197 offset:8192\n"
"s_waitcnt lgkmcnt(2)\n"
"v_mfma_f32_32x32x16_bf16 v[124:139], v[108:111], v[120:123], v[124:139]\n"
"ds_read_b64_tr_b16 v[104:105], v200 offset:8192\n"
"ds_read_b64_tr_b16 v[106:107], v200 offset:9216\n"
"s_waitcnt lgkmcnt(3)\n"
"v_mfma_f32_32x32x16_bf16 v[140:155], v[112:115], v[120:123], v[140:155]\n"
"ds_read_b128 v[92:95], v198 offset:0\n"
"ds_read_b128 v[96:99], v198 offset:4096\n"
"s_waitcnt lgkmcnt(4)\n"
"v_mfma_f32_32x32x16_bf16 v[156:171], v[116:119], v[120:123], v[156:171]\n"
"ds_read_b128 v[100:103], v198 offset:8192\n"
"s_waitcnt lgkmcnt(2)\n"
"v_mfma_f32_32x32x16_bf16 v[124:139], v[92:95], v[104:107], v[124:139]\n"
"ds_read_b64_tr_b16 v[120:121], v200 offset:12288\n"
"ds_read_b64_tr_b16 v[122:123], v200 offset:13312\n"
"s_waitcnt lgkmcnt(3)\n"
"v_mfma_f32_32x32x16_bf16 v[140:155], v[96:99], v[104:107], v[140:155]\n"
"ds_read_b128 v[108:111], v199 offset:0\n"
"ds_read_b128 v[112:115], v199 offset:4096\n"
"s_waitcnt lgkmcnt(4)\n"
"v_mfma_f32_32x32x16_bf16 v[156:171], v[100:103], v[104:107], v[156:171]\n"
"ds_read_b128 v[116:119], v199 offset:8192\n"
"s_waitcnt lgkmcnt(0)\n"
"v_mfma_f32_32x32x16_bf16 v[124:139], v[108:111], v[120:123], v[124:139]\n"
"s_barrier\n"
"ds_read_b64_tr_b16 v[104:105], v200 offset:28672\n"
"ds_read_b64_tr_b16 v[106:107], v200 offset:29696\n"
"v_mfma_f32_32x32x16_bf16 v[140:155], v[112:115], v[120:123], v[140:155]\n"
"ds_read_b128 v[92:95], v196 offset:28672\n"
"ds_read_b128 v[96:99], v196 offset:32768\n"
"v_mfma_f32_32x32x16_bf16 v[156:171], v[116:119], v[120:123], v[156:171]\n"
"ds_read_b128 v[100:103], v196 offset:36864\n"
"s_waitcnt lgkmcnt(2)\n"
"v_mfma_f32_32x32x16_bf16 v[124:139], v[92:95], v[104:107], v[124:139]\n"
"ds_read_b64_tr_b16 v[120:121], v200 offset:32768\n"
"ds_read_b64_tr_b16 v[122:123], v200 offset:33792\n"
"s_waitcnt lgkmcnt(3)\n"
"v_mfma_f32_32x32x16_bf16 v[140:155], v[96:99], v[104:107], v[140:155]\n"
"ds_read_b128 v[108:111], v197 offset:28672\n"
"ds_read_b128 v[112:115], v197 offset:32768\n"
"s_waitcnt lgkmcnt(4)\n"
"v_mfma_f32_32x32x16_bf16 v[156:171], v[100:103], v[104:107], v[156:171]\n"
"ds_read_b128 v[116:119], v197 offset:36864\n"
"s_waitcnt lgkmcnt(2)\n"
"v_mfma_f32_32x32x16_bf16 v[124:139], v[108:111], v[120:123], v[124:139]\n"
"ds_read_b64_tr_b16 v[104:105], v200 offset:36864\n"
"ds_read_b64_tr_b16 v[106:107], v200 offset:37888\n"
"s_waitcnt lgkmcnt(3)\n"
"v_mfma_f32_32x32x16_bf16 v[140:155], v[112:115], v[120:123], v[140:155]\n"
"ds_read_b128 v[92:95], v198 offset:28672\n"
"ds_read_b128 v[96:99], v198 offset:32768\n"
"s_waitcnt lgkmcnt(4)\n"
"v_mfma_f32_32x32x16_bf16 v[156:171], v[116:119], v[120:123], v[156:171]\n"
"ds_read_b128 v[100:103], v198 offset:36864\n"
"s_waitcnt lgkmcnt(2)\n"
"v_mfma_f32_32x32x16_bf16 v[124:139], v[92:95], v[104:107], v[124:139]\n"
"ds_read_b64_tr_b16 v[120:121], v200 offset:40960\n"
"ds_read_b64_tr_b16 v[122:123], v200 offset:41984\n"
"s_waitcnt lgkmcnt(3)\n"
"v_mfma_f32_32x32x16_bf16 v[140:155], v[96:99], v[104:107], v[140:155]\n"
"ds_read_b128 v[108:111], v199 offset:28672\n"
"ds_read_b128 v[112:115], v199 offset:32768\n"
"s_waitcnt lgkmcnt(4)\n"
"v_mfma_f32_32x32x16_bf16 v[156:171], v[100:103], v[104:107], v[156:171]\n"
"ds_read_b128 v[116:119], v199 offset:36864\n"
"s_waitcnt lgkmcnt(0)\n"
"v_mfma_f32_32x32x16_bf16 v[124:139], v[108:111], v[120:123], v[124:139]\n"
"s_barrier\n"
"ds_read_b64_tr_b16 v[104:105], v205 offset:0\n"
"ds_read_b64_tr_b16 v[106:107], v205 offset:1024\n"
"v_mfma_f32_32x32x16_bf16 v[140:155], v[112:115], v[120:123], v[140:155]\n"
"ds_read_b128 v[92:95], v201 offset:0\n"
"ds_read_b128 v[96:99], v201 offset:4096\n"
"v_mfma_f32_32x32x16_bf16 v[156:171], v[116:119], v[120:123], v[156:171]\n"
"ds_read_b128 v[100:103], v201 offset:8192\n"
"s_waitcnt lgkmcnt(2)\n"
"v_mfma_f32_32x32x16_bf16 v[124:139], v[92:95], v[104:107], v[124:139]\n"
"ds_read_b64_tr_b16 v[120:121], v205 offset:4096\n"
"ds_read_b64_tr_b16 v[122:123], v205 offset:5120\n"
"s_waitcnt lgkmcnt(3)\n"
"v_mfma_f32_32x32x16_bf16 v[140:155], v[96:99], v[104:107], v[140:155]\n"
"ds_read_b128 v[108:111], v202 offset:0\n"
"ds_read_b128 v[112:115], v202 offset:4096\n"
"s_waitcnt lgkmcnt(4)\n"
"v_mfma_f32_32x32x16_bf16 v[156:171], v[100:103], v[104:107], v[156:171]\n"
"ds_read_b128 v[116:119], v202 offset:8192\n"
"s_waitcnt lgkmcnt(2)\n"
"v_mfma_f32_32x32x16_bf16 v[124:139], v[108:111], v[120:123], v[124:139]\n"
"ds_read_b64_tr_b16 v[104:105], v205 offset:8192\n"
"ds_read_b64_tr_b16 v[106:107], v205 offset:9216\n"
"s_waitcnt lgkmcnt(3)\n"
"v_mfma_f32_32x32x16_bf16 v[140:155], v[112:115], v[120:123], v[140:155]\n"
"ds_read_b128 v[92:95], v203 offset:0\n"
"ds_read_b128 v[96:99], v203 offset:4096\n"
"s_waitcnt lgkmcnt(4)\n"
"v_mfma_f32_32x32x16_bf16 v[156:171], v[116:119], v[120:123], v[156:171]\n"
"ds_read_b128 v[100:103], v203 offset:8192\n"
"s_waitcnt lgkmcnt(2)\n"
"v_mfma_f32_32x32x16_bf16 v[124:139], v[92:95], v[104:107], v[124:139]\n"
"ds_read_b64_tr_b16 v[120:121], v205 offset:12288\n"
"ds_read_b64_tr_b16 v[122:123], v205 offset:13312\n"
"s_waitcnt lgkmcnt(3)\n"
"v_mfma_f32_32x32x16_bf16 v[140:155], v[96:99], v[104:107], v[140:155]\n"
"ds_read_b128 v[108:111], v204 offset:0\n"
"ds_read_b128 v[112:115], v204 offset:4096\n"
"s_waitcnt lgkmcnt(4)\n"
"v_mfma_f32_32x32x16_bf16 v[156:171], v[100:103], v[104:107], v[156:171]\n"
"ds_read_b128 v[116:119], v204 offset:8192\n"
"s_waitcnt lgkmcnt(0)\n"
"v_mfma_f32_32x32x16_bf16 v[124:139], v[108:111], v[120:123], v[124:139]\n"
"s_barrier\n"
"ds_read_b64_tr_b16 v[104:105], v205 offset:28672\n"
"ds_read_b64_tr_b16 v[106:107], v205 offset:29696\n"
"v_mfma_f32_32x32x16_bf16 v[140:155], v[112:115], v[120:123], v[140:155]\n"
"ds_read_b128 v[92:95], v201 offset:28672\n"
"ds_read_b128 v[96:99], v201 offset:32768\n"
"v_mfma_f32_32x32x16_bf16 v[156:171], v[116:119], v[120:123], v[156:171]\n"
"ds_read_b128 v[100:103], v201 offset:36864\n"
"s_waitcnt lgkmcnt(2)\n"
"v_mfma_f32_32x32x16_bf16 v[124:139], v[92:95], v[104:107], v[124:139]\n"
"ds_read_b64_tr_b16 v[120:121], v205 offset:32768\n"
"ds_read_b64_tr_b16 v[122:123], v205 offset:33792\n"
"s_waitcnt lgkmcnt(3)\n"
"v_mfma_f32_32x32x16_bf16 v[140:155], v[96:99], v[104:107], v[140:155]\n"
"ds_read_b128 v[108:111], v202 offset:28672\n"
"ds_read_b128 v[112:115], v202 offset:32768\n"
"s_waitcnt lgkmcnt(4)\n"
"v_mfma_f32_32x32x16_bf16 v[156:171], v[100:103], v[104:107], v[156:171]\n"
"ds_read_b128 v[116:119], v202 offset:36864\n"
"s_waitcnt lgkmcnt(2)\n"
"v_mfma_f32_32x32x16_bf16 v[124:139], v[108:111], v[120:123], v[124:139]\n"
"ds_read_b64_tr_b16 v[104:105], v205 offset:36864\n"
"ds_read_b64_tr_b16 v[106:107], v205 offset:37888\n"
"s_waitcnt lgkmcnt(3)\n"
"v_mfma_f32_32x32x16_bf16 v[140:155], v[112:115], v[120:123], v[140:155]\n"
"ds_read_b128 v[92:95], v203 offset:28672\n"
"ds_read_b128 v[96:99], v203 offset:32768\n"
"s_waitcnt lgkmcnt(4)\n"
"v_mfma_f32_32x32x16_bf16 v[156:171], v[116:119], v[120:123], v[156:171]\n"
"ds_read_b128 v[100:103], v203 offset:36864\n"
"s_waitcnt lgkmcnt(2)\n"
"v_mfma_f32_32x32x16_bf16 v[124:139], v[92:95], v[104:107], v[124:139]\n"
"ds_read_b64_tr_b16 v[120:121], v205 offset:40960\n"
"ds_read_b64_tr_b16 v[122:123], v205 offset:41984\n"
"s_waitcnt lgkmcnt(3)\n"
"v_mfma_f32_32x32x16_bf16 v[140:155], v[96:99], v[104:107], v[140:155]\n"
"ds_read_b128 v[108:111], v204 offset:28672\n"
"ds_read_b128 v[112:115], v204 offset:32768\n"
"s_waitcnt lgkmcnt(4)\n"
"v_mfma_f32_32x32x16_bf16 v[156:171], v[100:103], v[104:107], v[156:171]\n"
"ds_read_b128 v[116:119], v204 offset:36864\n"
"s_waitcnt lgkmcnt(0)\n"
"v_mfma_f32_32x32x16_bf16 v[124:139], v[108:111], v[120:123], v[124:139]\n"
"s_barrier\n"
"ds_read_b64_tr_b16 v[104:105], v200 offset:0\n"
"ds_read_b64_tr_b16 v[106:107], v200 offset:1024\n"
"v_mfma_f32_32x32x16_bf16 v[140:155], v[112:115], v[120:123], v[140:155]\n"
"ds_read_b128 v[92:95], v196 offset:0\n"
"ds_read_b128 v[96:99], v196 offset:4096\n"
"v_mfma_f32_32x32x16_bf16 v[156:171], v[116:119], v[120:123], v[156:171]\n"
"ds_read_b128 v[100:103], v196 offset:8192\n"
"s_waitcnt lgkmcnt(2)\n"
"v_mfma_f32_32x32x16_bf16 v[124:139], v[92:95], v[104:107], v[124:139]\n"
"ds_read_b64_tr_b16 v[120:121], v200 offset:4096\n"
"ds_read_b64_tr_b16 v[122:123], v200 offset:5120\n"
"s_waitcnt lgkmcnt(3)\n"
"v_mfma_f32_32x32x16_bf16 v[140:155], v[96:99], v[104:107], v[140:155]\n"
"ds_read_b128 v[108:111], v197 offset:0\n"
"ds_read_b128 v[112:115], v197 offset:4096\n"
"s_waitcnt lgkmcnt(4)\n"
"v_mfma_f32_32x32x16_bf16 v[156:171], v[100:103], v[104:107], v[156:171]\n"
"ds_read_b128 v[116:119], v197 offset:8192\n"
"s_waitcnt lgkmcnt(2)\n"
"v_mfma_f32_32x32x16_bf16 v[124:139], v[108:111], v[120:123], v[124:139]\n"
"ds_read_b64_tr_b16 v[104:105], v200 offset:8192\n"
"ds_read_b64_tr_b16 v[106:107], v200 offset:9216\n"
"s_waitcnt lgkmcnt(3)\n"
"v_mfma_f32_32x32x16_bf16 v[140:155], v[112:115], v[120:123], v[140:155]\n"
"ds_read_b128 v[92:95], v198 offset:0\n"
"ds_read_b128 v[96:99], v198 offset:4096\n"
"s_waitcnt lgkmcnt(4)\n"
"v_mfma_f32_32x32x16_bf16 v[156:171], v[116:119], v[120:123], v[156:171]\n"
"ds_read_b128 v[100:103], v198 offset:8192\n"
"s_waitcnt lgkmcnt(2)\n"
"v_mfma_f32_32x32x16_bf16 v[124:139], v[92:95], v[104:107], v[124:139]\n"
"ds_read_b64_tr_b16 v[120:121], v200 offset:12288\n"
"ds_read_b64_tr_b16 v[122:123], v200 offset:13312\n"
"s_waitcnt lgkmcnt(3)\n"
"v_mfma_f32_32x32x16_bf16 v[140:155], v[96:99], v[104:107], v[140:155]\n"
"ds_read_b128 v[108:111], v199 offset:0\n"
"ds_read_b128 v[112:115], v199 offset:4096\n"
"s_waitcnt lgkmcnt(4)\n"
"v_mfma_f32_32x32x16_bf16 v[156:171], v[100:103], v[104:107], v[156:171]\n"
"ds_read_b128 v[116:119], v199 offset:8192\n"
"s_waitcnt lgkmcnt(0)\n"
"v_mfma_f32_32x32x16_bf16 v[124:139], v[108:111], v[120:123], v[124:139]\n"
"v_mfma_f32_32x32x16_bf16 v[140:155], v[112:115], v[120:123], v[140:155]\n"
"v_mfma_f32_32x32x16_bf16 v[156:171], v[116:119], v[120:123], v[156:171]\n"
"s_add_u32 s50, s58, 0\n"
"s_add_u32 s51, s50, 28672\n"
"s_add_u32 s52, s50, 57344\n"
"s_cmp_eq_u32 s41, 21\n"
"s_cselect_b32 s51, 2130706432, s51\n"
"s_cselect_b32 s52, 2130706432, s52\n"
"s_nop 3\n"
"v_cvt_pk_bf16_f32 v220, v124, v125\n"
"v_cvt_pk_bf16_f32 v221, v126, v127\n"
"v_cvt_pk_bf16_f32 v222, v128, v129\n"
"v_cvt_pk_bf16_f32 v223, v130, v131\n"
"v_cvt_pk_bf16_f32 v224, v132, v133\n"
"v_cvt_pk_bf16_f32 v225, v134, v135\n"
"v_cvt_pk_bf16_f32 v226, v136, v137\n"
"v_cvt_pk_bf16_f32 v227, v138, v139\n"
"v_permlane32_swap_b32 v220, v222\n"
"v_permlane32_swap_b32 v221, v223\n"
"v_permlane32_swap_b32 v224, v226\n"
"v_permlane32_swap_b32 v225, v227\n"
"buffer_store_dwordx4 v[220:223], v206, s[20:23], s50 offen sc1\n"
"buffer_store_dwordx4 v[224:227], v207, s[20:23], s50 offen sc1\n"
"v_cvt_pk_bf16_f32 v228, v140, v141\n"
"v_cvt_pk_bf16_f32 v229, v142, v143\n"
"v_cvt_pk_bf16_f32 v230, v144, v145\n"
"v_cvt_pk_bf16_f32 v231, v146, v147\n"
"v_cvt_pk_bf16_f32 v232, v148, v149\n"
"v_cvt_pk_bf16_f32 v233, v150, v151\n"
"v_cvt_pk_bf16_f32 v234, v152, v153\n"
"v_cvt_pk_bf16_f32 v235, v154, v155\n"
"v_permlane32_swap_b32 v228, v230\n"
"v_permlane32_swap_b32 v229, v231\n"
"v_permlane32_swap_b32 v232, v234\n"
"v_permlane32_swap_b32 v233, v235\n"
"buffer_store_dwordx4 v[228:231], v206, s[20:23], s51 offen sc1\n"
"buffer_store_dwordx4 v[232:235], v207, s[20:23], s51 offen sc1\n"
"v_cvt_pk_bf16_f32 v236, v156, v157\n"
"v_cvt_pk_bf16_f32 v237, v158, v159\n"
"v_cvt_pk_bf16_f32 v238, v160, v161\n"
"v_cvt_pk_bf16_f32 v239, v162, v163\n"
"v_cvt_pk_bf16_f32 v240, v164, v165\n"
"v_cvt_pk_bf16_f32 v241, v166, v167\n"
"v_cvt_pk_bf16_f32 v242, v168, v169\n"
"v_cvt_pk_bf16_f32 v243, v170, v171\n"
"v_permlane32_swap_b32 v236, v238\n"
"v_permlane32_swap_b32 v237, v239\n"
"v_permlane32_swap_b32 v240, v242\n"
"v_permlane32_swap_b32 v241, v243\n"
"buffer_store_dwordx4 v[236:239], v206, s[20:23], s52 offen sc1\n"
"buffer_store_dwordx4 v[240:243], v207, s[20:23], s52 offen sc1\n"
"s_barrier\n"
"ds_read_b64_tr_b16 v[104:105], v200 offset:28672\n"
"ds_read_b64_tr_b16 v[106:107], v200 offset:29696\n"
"ds_read_b128 v[92:95], v196 offset:28672\n"
"ds_read_b128 v[96:99], v196 offset:32768\n"
"ds_read_b128 v[100:103], v196 offset:36864\n"
"s_waitcnt lgkmcnt(2)\n"
"v_mfma_f32_32x32x16_bf16 v[124:139], v[92:95], v[104:107], 0\n"
"ds_read_b64_tr_b16 v[120:121], v200 offset:32768\n"
"ds_read_b64_tr_b16 v[122:123], v200 offset:33792\n"
"s_waitcnt lgkmcnt(3)\n"
"v_mfma_f32_32x32x16_bf16 v[140:155], v[96:99], v[104:107], 0\n"
"ds_read_b128 v[108:111], v197 offset:28672\n"
"ds_read_b128 v[112:115], v197 offset:32768\n"
"s_waitcnt lgkmcnt(4)\n"
"v_mfma_f32_32x32x16_bf16 v[156:171], v[100:103], v[104:107], 0\n"
"ds_read_b128 v[116:119], v197 offset:36864\n"
"s_waitcnt lgkmcnt(2)\n"
"v_mfma_f32_32x32x16_bf16 v[124:139], v[108:111], v[120:123], v[124:139]\n"
"ds_read_b64_tr_b16 v[104:105], v200 offset:36864\n"
"ds_read_b64_tr_b16 v[106:107], v200 offset:37888\n"
"s_waitcnt lgkmcnt(3)\n"
"v_mfma_f32_32x32x16_bf16 v[140:155], v[112:115], v[120:123], v[140:155]\n"
"ds_read_b128 v[92:95], v198 offset:28672\n"
"ds_read_b128 v[96:99], v198 offset:32768\n"
"s_waitcnt lgkmcnt(4)\n"
"v_mfma_f32_32x32x16_bf16 v[156:171], v[116:119], v[120:123], v[156:171]\n"
"ds_read_b128 v[100:103], v198 offset:36864\n"
"s_waitcnt lgkmcnt(2)\n"
"v_mfma_f32_32x32x16_bf16 v[124:139], v[92:95], v[104:107], v[124:139]\n"
"ds_read_b64_tr_b16 v[120:121], v200 offset:40960\n"
"ds_read_b64_tr_b16 v[122:123], v200 offset:41984\n"
"s_waitcnt lgkmcnt(3)\n"
"v_mfma_f32_32x32x16_bf16 v[140:155], v[96:99], v[104:107], v[140:155]\n"
"ds_read_b128 v[108:111], v199 offset:28672\n"
"ds_read_b128 v[112:115], v199 offset:32768\n"
"s_waitcnt lgkmcnt(4)\n"
"v_mfma_f32_32x32x16_bf16 v[156:171], v[100:103], v[104:107], v[156:171]\n"
"ds_read_b128 v[116:119], v199 offset:36864\n"
"s_waitcnt lgkmcnt(0)\n"
"v_mfma_f32_32x32x16_bf16 v[124:139], v[108:111], v[120:123], v[124:139]\n"
"s_barrier\n"
"ds_read_b64_tr_b16 v[104:105], v205 offset:0\n"
"ds_read_b64_tr_b16 v[106:107], v205 offset:1024\n"
"v_mfma_f32_32x32x16_bf16 v[140:155], v[112:115], v[120:123], v[140:155]\n"
"ds_read_b128 v[92:95], v201 offset:0\n"
"ds_read_b128 v[96:99], v201 offset:4096\n"
"v_mfma_f32_32x32x16_bf16 v[156:171], v[116:119], v[120:123], v[156:171]\n"
"ds_read_b128 v[100:103], v201 offset:8192\n"
"s_waitcnt lgkmcnt(2)\n"
"v_mfma_f32_32x32x16_bf16 v[124:139], v[92:95], v[104:107], v[124:139]\n"
"ds_read_b64_tr_b16 v[120:121], v205 offset:4096\n"
"ds_read_b64_tr_b16 v[122:123], v205 offset:5120\n"
"s_waitcnt lgkmcnt(3)\n"
"v_mfma_f32_32x32x16_bf16 v[140:155], v[96:99], v[104:107], v[140:155]\n"
"ds_read_b128 v[108:111], v202 offset:0\n"
"ds_read_b128 v[112:115], v202 offset:4096\n"
"s_waitcnt lgkmcnt(4)\n"
"v_mfma_f32_32x32x16_bf16 v[156:171], v[100:103], v[104:107], v[156:171]\n"
"ds_read_b128 v[116:119], v202 offset:8192\n"
"s_waitcnt lgkmcnt(2)\n"
"v_mfma_f32_32x32x16_bf16 v[124:139], v[108:111], v[120:123], v[124:139]\n"
"ds_read_b64_tr_b16 v[104:105], v205 offset:8192\n"
"ds_read_b64_tr_b16 v[106:107], v205 offset:9216\n"
"s_waitcnt lgkmcnt(3)\n"
"v_mfma_f32_32x32x16_bf16 v[140:155], v[112:115], v[120:123], v[140:155]\n"
"ds_read_b128 v[92:95], v203 offset:0\n"
"ds_read_b128 v[96:99], v203 offset:4096\n"
"s_waitcnt lgkmcnt(4)\n"
"v_mfma_f32_32x32x16_bf16 v[156:171], v[116:119], v[120:123], v[156:171]\n"
"ds_read_b128 v[100:103], v203 offset:8192\n"
"s_waitcnt lgkmcnt(2)\n"
"v_mfma_f32_32x32x16_bf16 v[124:139], v[92:95], v[104:107], v[124:139]\n"
"ds_read_b64_tr_b16 v[120:121], v205 offset:12288\n"
"ds_read_b64_tr_b16 v[122:123], v205 offset:13312\n"
"s_waitcnt lgkmcnt(3)\n"
"v_mfma_f32_32x32x16_bf16 v[140:155], v[96:99], v[104:107], v[140:155]\n"
"ds_read_b128 v[108:111], v204 offset:0\n"
"ds_read_b128 v[112:115], v204 offset:4096\n"
"s_waitcnt lgkmcnt(4)\n"
"v_mfma_f32_32x32x16_bf16 v[156:171], v[100:103], v[104:107], v[156:171]\n"
"ds_read_b128 v[116:119], v204 offset:8192\n"
"s_waitcnt lgkmcnt(0)\n"
"v_mfma_f32_32x32x16_bf16 v[124:139], v[108:111], v[120:123], v[124:139]\n"
"s_waitcnt vmcnt(0)\n"
"s_barrier\n"
"s_barrier\n"
"ds_read_b64_tr_b16 v[104:105], v205 offset:28672\n"
"ds_read_b64_tr_b16 v[106:107], v205 offset:29696\n"
"v_mfma_f32_32x32x16_bf16 v[140:155], v[112:115], v[120:123], v[140:155]\n"
"ds_read_b128 v[92:95], v201 offset:28672\n"
"ds_read_b128 v[96:99], v201 offset:32768\n"
"v_mfma_f32_32x32x16_bf16 v[156:171], v[116:119], v[120:123], v[156:171]\n"
"ds_read_b128 v[100:103], v201 offset:36864\n"
"s_waitcnt lgkmcnt(2)\n"
"v_mfma_f32_32x32x16_bf16 v[124:139], v[92:95], v[104:107], v[124:139]\n"
"ds_read_b64_tr_b16 v[120:121], v205 offset:32768\n"
"ds_read_b64_tr_b16 v[122:123], v205 offset:33792\n"
"s_waitcnt lgkmcnt(3)\n"
"v_mfma_f32_32x32x16_bf16 v[140:155], v[96:99], v[104:107], v[140:155]\n"
"ds_read_b128 v[108:111], v202 offset:28672\n"
"ds_read_b128 v[112:115], v202 offset:32768\n"
"s_waitcnt lgkmcnt(4)\n"
"v_mfma_f32_32x32x16_bf16 v[156:171], v[100:103], v[104:107], v[156:171]\n"
"ds_read_b128 v[116:119], v202 offset:36864\n"
"s_waitcnt lgkmcnt(2)\n"
"v_mfma_f32_32x32x16_bf16 v[124:139], v[108:111], v[120:123], v[124:139]\n"
"ds_read_b64_tr_b16 v[104:105], v205 offset:36864\n"
"ds_read_b64_tr_b16 v[106:107], v205 offset:37888\n"
"s_waitcnt lgkmcnt(3)\n"
"v_mfma_f32_32x32x16_bf16 v[140:155], v[112:115], v[120:123], v[140:155]\n"
"ds_read_b128 v[92:95], v203 offset:28672\n"
"ds_read_b128 v[96:99], v203 offset:32768\n"
"s_waitcnt lgkmcnt(4)\n"
"v_mfma_f32_32x32x16_bf16 v[156:171], v[116:119], v[120:123], v[156:171]\n"
"ds_read_b128 v[100:103], v203 offset:36864\n"
"s_waitcnt lgkmcnt(2)\n"
"v_mfma_f32_32x32x16_bf16 v[124:139], v[92:95], v[104:107], v[124:139]\n"
"ds_read_b64_tr_b16 v[120:121], v205 offset:40960\n"
"ds_read_b64_tr_b16 v[122:123], v205 offset:41984\n"
"s_waitcnt lgkmcnt(3)\n"
"v_mfma_f32_32x32x16_bf16 v[140:155], v[96:99], v[104:107], v[140:155]\n"
"ds_read_b128 v[108:111], v204 offset:28672\n"
"ds_read_b128 v[112:115], v204 offset:32768\n"
"s_waitcnt lgkmcnt(4)\n"
"v_mfma_f32_32x32x16_bf16 v[156:171], v[100:103], v[104:107], v[156:171]\n"
"ds_read_b128 v[116:119], v204 offset:36864\n"
"s_waitcnt lgkmcnt(0)\n"
"v_mfma_f32_32x32x16_bf16 v[124:139], v[108:111], v[120:123], v[124:139]\n"
"s_barrier\n"
"ds_read_b64_tr_b16 v[104:105], v200 offset:0\n"
"ds_read_b64_tr_b16 v[106:107], v200 offset:1024\n"
"v_mfma_f32_32x32x16_bf16 v[140:155], v[112:115], v[120:123], v[140:155]\n"
"ds_read_b128 v[92:95], v196 offset:0\n"
"ds_read_b128 v[96:99], v196 offset:4096\n"
"v_mfma_f32_32x32x16_bf16 v[156:171], v[116:119], v[120:123], v[156:171]\n"
"ds_read_b128 v[100:103], v196 offset:8192\n"
"s_waitcnt lgkmcnt(2)\n"
"v_mfma_f32_32x32x16_bf16 v[124:139], v[92:95], v[104:107], v[124:139]\n"
"ds_read_b64_tr_b16 v[120:121], v200 offset:4096\n"
"ds_read_b64_tr_b16 v[122:123], v200 offset:5120\n"
"s_waitcnt lgkmcnt(3)\n"
"v_mfma_f32_32x32x16_bf16 v[140:155], v[96:99], v[104:107], v[140:155]\n"
"ds_read_b128 v[108:111], v197 offset:0\n"
"ds_read_b128 v[112:115], v197 offset:4096\n"
"s_waitcnt lgkmcnt(4)\n"
"v_mfma_f32_32x32x16_bf16 v[156:171], v[100:103], v[104:107], v[156:171]\n"
"ds_read_b128 v[116:119], v197 offset:8192\n"
"s_waitcnt lgkmcnt(2)\n"
"v_mfma_f32_32x32x16_bf16 v[124:139], v[108:111], v[120:123], v[124:139]\n"
"ds_read_b64_tr_b16 v[104:105], v200 offset:8192\n"
"ds_read_b64_tr_b16 v[106:107], v200 offset:9216\n"
"s_waitcnt lgkmcnt(3)\n"
"v_mfma_f32_32x32x16_bf16 v[140:155], v[112:115], v[120:123], v[140:155]\n"
"ds_read_b128 v[92:95], v198 offset:0\n"
"ds_read_b128 v[96:99], v198 offset:4096\n"
"s_waitcnt lgkmcnt(4)\n"
"v_mfma_f32_32x32x16_bf16 v[156:171], v[116:119], v[120:123], v[156:171]\n"
"ds_read_b128 v[100:103], v198 offset:8192\n"
"s_waitcnt lgkmcnt(2)\n"
"v_mfma_f32_32x32x16_bf16 v[124:139], v[92:95], v[104:107], v[124:139]\n"
"ds_read_b64_tr_b16 v[120:121], v200 offset:12288\n"
"ds_read_b64_tr_b16 v[122:123], v200 offset:13312\n"
"s_waitcnt lgkmcnt(3)\n"
"v_mfma_f32_32x32x16_bf16 v[140:155], v[96:99], v[104:107], v[140:155]\n"
"ds_read_b128 v[108:111], v199 offset:0\n"
"ds_read_b128 v[112:115], v199 offset:4096\n"
"s_waitcnt lgkmcnt(4)\n"
"v_mfma_f32_32x32x16_bf16 v[156:171], v[100:103], v[104:107], v[156:171]\n"
"ds_read_b128 v[116:119], v199 offset:8192\n"
"s_waitcnt lgkmcnt(0)\n"
"v_mfma_f32_32x32x16_bf16 v[124:139], v[108:111], v[120:123], v[124:139]\n"
"s_barrier\n"
"ds_read_b64_tr_b16 v[104:105], v200 offset:28672\n"
"ds_read_b64_tr_b16 v[106:107], v200 offset:29696\n"
"v_mfma_f32_32x32x16_bf16 v[140:155], v[112:115], v[120:123], v[140:155]\n"
"ds_read_b128 v[92:95], v196 offset:28672\n"
"ds_read_b128 v[96:99], v196 offset:32768\n"
"v_mfma_f32_32x32x16_bf16 v[156:171], v[116:119], v[120:123], v[156:171]\n"
"ds_read_b128 v[100:103], v196 offset:36864\n"
"s_waitcnt lgkmcnt(2)\n"
"v_mfma_f32_32x32x16_bf16 v[124:139], v[92:95], v[104:107], v[124:139]\n"
"ds_read_b64_tr_b16 v[120:121], v200 offset:32768\n"
"ds_read_b64_tr_b16 v[122:123], v200 offset:33792\n"
"s_waitcnt lgkmcnt(3)\n"
"v_mfma_f32_32x32x16_bf16 v[140:155], v[96:99], v[104:107], v[140:155]\n"
"ds_read_b128 v[108:111], v197 offset:28672\n"
"ds_read_b128 v[112:115], v197 offset:32768\n"
"s_waitcnt lgkmcnt(4)\n"
"v_mfma_f32_32x32x16_bf16 v[156:171], v[100:103], v[104:107], v[156:171]\n"
"ds_read_b128 v[116:119], v197 offset:36864\n"
"s_waitcnt lgkmcnt(2)\n"
"v_mfma_f32_32x32x16_bf16 v[124:139], v[108:111], v[120:123], v[124:139]\n"
"ds_read_b64_tr_b16 v[104:105], v200 offset:36864\n"
"ds_read_b64_tr_b16 v[106:107], v200 offset:37888\n"
"s_waitcnt lgkmcnt(3)\n"
"v_mfma_f32_32x32x16_bf16 v[140:155], v[112:115], v[120:123], v[140:155]\n"
"ds_read_b128 v[92:95], v198 offset:28672\n"
"ds_read_b128 v[96:99], v198 offset:32768\n"
"s_waitcnt lgkmcnt(4)\n"
"v_mfma_f32_32x32x16_bf16 v[156:171], v[116:119], v[120:123], v[156:171]\n"
"ds_read_b128 v[100:103], v198 offset:36864\n"
"s_waitcnt lgkmcnt(2)\n"
"v_mfma_f32_32x32x16_bf16 v[124:139], v[92:95], v[104:107], v[124:139]\n"
"ds_read_b64_tr_b16 v[120:121], v200 offset:40960\n"
"ds_read_b64_tr_b16 v[122:123], v200 offset:41984\n"
"s_waitcnt lgkmcnt(3)\n"
"v_mfma_f32_32x32x16_bf16 v[140:155], v[96:99], v[104:107], v[140:155]\n"
"ds_read_b128 v[108:111], v199 offset:28672\n"
"ds_read_b128 v[112:115], v199 offset:32768\n"
"s_waitcnt lgkmcnt(4)\n"
"v_mfma_f32_32x32x16_bf16 v[156:171], v[100:103], v[104:107], v[156:171]\n"
"ds_read_b128 v[116:119], v199 offset:36864\n"
"s_waitcnt lgkmcnt(0)\n"
"v_mfma_f32_32x32x16_bf16 v[124:139], v[108:111], v[120:123], v[124:139]\n"
"s_barrier\n"
"ds_read_b64_tr_b16 v[104:105], v205 offset:0\n"
"ds_read_b64_tr_b16 v[106:107], v205 offset:1024\n"
"v_mfma_f32_32x32x16_bf16 v[140:155], v[112:115], v[120:123], v[140:155]\n"
"ds_read_b128 v[92:95], v201 offset:0\n"
"ds_read_b128 v[96:99], v201 offset:4096\n"
"v_mfma_f32_32x32x16_bf16 v[156:171], v[116:119], v[120:123], v[156:171]\n"
"ds_read_b128 v[100:103], v201 offset:8192\n"
"s_waitcnt lgkmcnt(2)\n"
"v_mfma_f32_32x32x16_bf16 v[124:139], v[92:95], v[104:107], v[124:139]\n"
"ds_read_b64_tr_b16 v[120:121], v205 offset:4096\n"
"ds_read_b64_tr_b16 v[122:123], v205 offset:5120\n"
"s_waitcnt lgkmcnt(3)\n"
"v_mfma_f32_32x32x16_bf16 v[140:155], v[96:99], v[104:107], v[140:155]\n"
"ds_read_b128 v[108:111], v202 offset:0\n"
"ds_read_b128 v[112:115], v202 offset:4096\n"
"s_waitcnt lgkmcnt(4)\n"
"v_mfma_f32_32x32x16_bf16 v[156:171], v[100:103], v[104:107], v[156:171]\n"
"ds_read_b128 v[116:119], v202 offset:8192\n"
"s_waitcnt lgkmcnt(2)\n"
"v_mfma_f32_32x32x16_bf16 v[124:139], v[108:111], v[120:123], v[124:139]\n"
"ds_read_b64_tr_b16 v[104:105], v205 offset:8192\n"
"ds_read_b64_tr_b16 v[106:107], v205 offset:9216\n"
"s_waitcnt lgkmcnt(3)\n"
"v_mfma_f32_32x32x16_bf16 v[140:155], v[112:115], v[120:123], v[140:155]\n"
"ds_read_b128 v[92:95], v203 offset:0\n"
"ds_read_b128 v[96:99], v203 offset:4096\n"
"s_waitcnt lgkmcnt(4)\n"
"v_mfma_f32_32x32x16_bf16 v[156:171], v[116:119], v[120:123], v[156:171]\n"
"ds_read_b128 v[100:103], v203 offset:8192\n"
"s_waitcnt lgkmcnt(2)\n"
"v_mfma_f32_32x32x16_bf16 v[124:139], v[92:95], v[104:107], v[124:139]\n"
"ds_read_b64_tr_b16 v[120:121], v205 offset:12288\n"
"ds_read_b64_tr_b16 v[122:123], v205 offset:13312\n"
"s_waitcnt lgkmcnt(3)\n"
"v_mfma_f32_32x32x16_bf16 v[140:155], v[96:99], v[104:107], v[140:155]\n"
"ds_read_b128 v[108:111], v204 offset:0\n"
"ds_read_b128 v[112:115], v204 offset:4096\n"
"s_waitcnt lgkmcnt(4)\n"
"v_mfma_f32_32x32x16_bf16 v[156:171], v[100:103], v[104:107], v[156:171]\n"
"ds_read_b128 v[116:119], v204 offset:8192\n"
"s_waitcnt lgkmcnt(0)\n"
"v_mfma_f32_32x32x16_bf16 v[124:139], v[108:111], v[120:123], v[124:139]\n"
"s_barrier\n"
"ds_read_b64_tr_b16 v[104:105], v205 offset:28672\n"
"ds_read_b64_tr_b16 v[106:107], v205 offset:29696\n"
"v_mfma_f32_32x32x16_bf16 v[140:155], v[112:115], v[120:123], v[140:155]\n"
"ds_read_b128 v[92:95], v201 offset:28672\n"
"ds_read_b128 v[96:99], v201 offset:32768\n"
"v_mfma_f32_32x32x16_bf16 v[156:171], v[116:119], v[120:123], v[156:171]\n"
"ds_read_b128 v[100:103], v201 offset:36864\n"
"s_waitcnt lgkmcnt(2)\n"
"v_mfma_f32_32x32x16_bf16 v[124:139], v[92:95], v[104:107], v[124:139]\n"
"ds_read_b64_tr_b16 v[120:121], v205 offset:32768\n"
"ds_read_b64_tr_b16 v[122:123], v205 offset:33792\n"
"s_waitcnt lgkmcnt(3)\n"
"v_mfma_f32_32x32x16_bf16 v[140:155], v[96:99], v[104:107], v[140:155]\n"
"ds_read_b128 v[108:111], v202 offset:28672\n"
"ds_read_b128 v[112:115], v202 offset:32768\n"
"s_waitcnt lgkmcnt(4)\n"
"v_mfma_f32_32x32x16_bf16 v[156:171], v[100:103], v[104:107], v[156:171]\n"
"ds_read_b128 v[116:119], v202 offset:36864\n"
"s_waitcnt lgkmcnt(2)\n"
"v_mfma_f32_32x32x16_bf16 v[124:139], v[108:111], v[120:123], v[124:139]\n"
"ds_read_b64_tr_b16 v[104:105], v205 offset:36864\n"
"ds_read_b64_tr_b16 v[106:107], v205 offset:37888\n"
"s_waitcnt lgkmcnt(3)\n"
"v_mfma_f32_32x32x16_bf16 v[140:155], v[112:115], v[120:123], v[140:155]\n"
"ds_read_b128 v[92:95], v203 offset:28672\n"
"ds_read_b128 v[96:99], v203 offset:32768\n"
"s_waitcnt lgkmcnt(4)\n"
"v_mfma_f32_32x32x16_bf16 v[156:171], v[116:119], v[120:123], v[156:171]\n"
"ds_read_b128 v[100:103], v203 offset:36864\n"
"s_waitcnt lgkmcnt(2)\n"
"v_mfma_f32_32x32x16_bf16 v[124:139], v[92:95], v[104:107], v[124:139]\n"
"ds_read_b64_tr_b16 v[120:121], v205 offset:40960\n"
"ds_read_b64_tr_b16 v[122:123], v205 offset:41984\n"
"s_waitcnt lgkmcnt(3)\n"
"v_mfma_f32_32x32x16_bf16 v[140:155], v[96:99], v[104:107], v[140:155]\n"
"ds_read_b128 v[108:111], v204 offset:28672\n"
"ds_read_b128 v[112:115], v204 offset:32768\n"
"s_waitcnt lgkmcnt(4)\n"
"v_mfma_f32_32x32x16_bf16 v[156:171], v[100:103], v[104:107], v[156:171]\n"
"ds_read_b128 v[116:119], v204 offset:36864\n"
"s_waitcnt lgkmcnt(0)\n"
"v_mfma_f32_32x32x16_bf16 v[124:139], v[108:111], v[120:123], v[124:139]\n"
"v_mfma_f32_32x32x16_bf16 v[140:155], v[112:115], v[120:123], v[140:155]\n"
"v_mfma_f32_32x32x16_bf16 v[156:171], v[116:119], v[120:123], v[156:171]\n"
"s_nop 9\n"
"v_cvt_pk_bf16_f32 v220, v124, v125\n"
"v_cvt_pk_bf16_f32 v221, v126, v127\n"
"v_cvt_pk_bf16_f32 v222, v128, v129\n"
"v_cvt_pk_bf16_f32 v223, v130, v131\n"
"v_cvt_pk_bf16_f32 v224, v132, v133\n"
"v_cvt_pk_bf16_f32 v225, v134, v135\n"
"v_cvt_pk_bf16_f32 v226, v136, v137\n"
"v_cvt_pk_bf16_f32 v227, v138, v139\n"
"v_permlane32_swap_b32 v220, v222\n"
"v_permlane32_swap_b32 v221, v223\n"
"v_permlane32_swap_b32 v224, v226\n"
"v_permlane32_swap_b32 v225, v227\n"
"v_cvt_pk_bf16_f32 v228, v140, v141\n"
"v_cvt_pk_bf16_f32 v229, v142, v143\n"
"v_cvt_pk_bf16_f32 v230, v144, v145\n"
"v_cvt_pk_bf16_f32 v231, v146, v147\n"
"v_cvt_pk_bf16_f32 v232, v148, v149\n"
"v_cvt_pk_bf16_f32 v233, v150, v151\n"
"v_cvt_pk_bf16_f32 v234, v152, v153\n"
"v_cvt_pk_bf16_f32 v235, v154, v155\n"
"v_permlane32_swap_b32 v228, v230\n"
"v_permlane32_swap_b32 v229, v231\n"
"v_permlane32_swap_b32 v232, v234\n"
"v_permlane32_swap_b32 v233, v235\n"
"v_cvt_pk_bf16_f32 v236, v156, v157\n"
"v_cvt_pk_bf16_f32 v237, v158, v159\n"
"v_cvt_pk_bf16_f32 v238, v160, v161\n"
"v_cvt_pk_bf16_f32 v239, v162, v163\n"
"v_cvt_pk_bf16_f32 v240, v164, v165\n"
"v_cvt_pk_bf16_f32 v241, v166, v167\n"
"v_cvt_pk_bf16_f32 v242, v168, v169\n"
"v_cvt_pk_bf16_f32 v243, v170, v171\n"
"v_permlane32_swap_b32 v236, v238\n"
"v_permlane32_swap_b32 v237, v239\n"
"v_permlane32_swap_b32 v240, v242\n"
"v_permlane32_swap_b32 v241, v243\n"
"s_branch .Lfused_fwd_pro_c\n"
".Lfused_fwd_idle:\n"
"s_xor_b32 s58, s40, 31\n"
"s_lshl_b32 s58, s58, 3\n"
"s_add_u32 s58, s58, s39\n"
"s_lshl_b32 s52, s58, 3\n"
"s_add_u32 s52, s52, s38\n"
"s_lshl_b32 s52, s52, 14\n"
"v_mov_b32 v92, -1\n"
"v_mov_b32 v93, -1\n"
"v_mov_b32 v94, -1\n"
"v_mov_b32 v95, -1\n"
"s_add_u32 s53, s52, 14680064\n"
"s_add_u32 s52, s52, 12582912\n"
"s_waitcnt lgkmcnt(0)\n"
"s_add_u32 s50, s20, 16777216\n"
"s_addc_u32 s51, s21, 0\n"
"s_load_dwordx2 s[36:37], s[50:51], 0x0\n"
"buffer_store_dwordx4 v[92:95], v4, s[20:23], s52 offen offset:0 sc1\n"
"buffer_store_dwordx4 v[92:95], v4, s[20:23], s52 offen offset:1024 sc1\n"
"buffer_store_dwordx4 v[92:95], v4, s[20:23], s52 offen offset:2048 sc1\n"
"buffer_store_dwordx4 v[92:95], v4, s[20:23], s52 offen offset:3072 sc1\n"
"s_add_u32 s52, s52, 4096\n"
"buffer_store_dwordx4 v[92:95], v4, s[20:23], s53 offen offset:0 sc1\n"
"buffer_store_dwordx4 v[92:95], v4, s[20:23], s53 offen offset:1024 sc1\n"
"buffer_store_dwordx4 v[92:95], v4, s[20:23], s53 offen offset:2048 sc1\n"
"buffer_store_dwordx4 v[92:95], v4, s[20:23], s53 offen offset:3072 sc1\n"
"s_add_u32 s53, s53, 4096\n"
"buffer_store_dwordx4 v[92:95], v4, s[20:23], s52 offen offset:0 sc1\n"
"buffer_store_dwordx4 v[92:95], v4, s[20:23], s52 offen offset:1024 sc1\n"
"buffer_store_dwordx4 v[92:95], v4, s[20:23], s52 offen offset:2048 sc1\n"
"buffer_store_dwordx4 v[92:95], v4, s[20:23], s52 offen offset:3072 sc1\n"
"s_add_u32 s52, s52, 4096\n"
"buffer_store_dwordx4 v[92:95], v4, s[20:23], s53 offen offset:0 sc1\n"
"buffer_store_dwordx4 v[92:95], v4, s[20:23], s53 offen offset:1024 sc1\n"
"buffer_store_dwordx4 v[92:95], v4, s[20:23], s53 offen offset:2048 sc1\n"
"buffer_store_dwordx4 v[92:95], v4, s[20:23], s53 offen offset:3072 sc1\n"
"s_add_u32 s53, s53, 4096\n"
"buffer_store_dwordx4 v[92:95], v4, s[20:23], s52 offen offset:0 sc1\n"
"buffer_store_dwordx4 v[92:95], v4, s[20:23], s52 offen offset:1024 sc1\n"
"buffer_store_dwordx4 v[92:95], v4, s[20:23], s52 offen offset:2048 sc1\n"
"buffer_store_dwordx4 v[92:95], v4, s[20:23], s52 offen offset:3072 sc1\n"
"s_add_u32 s52, s52, 4096\n"
"buffer_store_dwordx4 v[92:95], v4, s[20:23], s53 offen offset:0 sc1\n"
"buffer_store_dwordx4 v[92:95], v4, s[20:23], s53 offen offset:1024 sc1\n"
"buffer_store_dwordx4 v[92:95], v4, s[20:23], s53 offen offset:2048 sc1\n"
"buffer_store_dwordx4 v[92:95], v4, s[20:23], s53 offen offset:3072 sc1\n"
"s_add_u32 s53, s53, 4096\n"
"buffer_store_dwordx4 v[92:95], v4, s[20:23], s52 offen offset:0 sc1\n"
"buffer_store_dwordx4 v[92:95], v4, s[20:23], s52 offen offset:1024 sc1\n"
"buffer_store_dwordx4 v[92:95], v4, s[20:23], s52 offen offset:2048 sc1\n"
"buffer_store_dwordx4 v[92:95], v4, s[20:23], s52 offen offset:3072 sc1\n"
"buffer_store_dwordx4 v[92:95], v4, s[20:23], s53 offen offset:0 sc1\n"
"buffer_store_dwordx4 v[92:95], v4, s[20:23], s53 offen offset:1024 sc1\n"
"buffer_store_dwordx4 v[92:95], v4, s[20:23], s53 offen offset:2048 sc1\n"
"buffer_store_dwordx4 v[92:95], v4, s[20:23], s53 offen offset:3072 sc1\n"
"s_and_b32 s43, s40, 1\n"
"s_lshr_b32 s44, s40, 1\n"
"s_mul_i32 s47, s39, 3584\n"
"s_mul_i32 s52, s43, 1536\n"
"s_add_u32 s47, s47, s52\n"
"s_lshr_b32 s45, s38, 2\n"
"s_and_b32 s46, s38, 3\n"
"s_lshl_b32 s48, s38, 10\n"
"s_lshl_b32 s52, s44, 2\n"
"s_add_u32 s52, s52, s46\n"
"s_mul_i32 s49, s52, 28672\n"
"s_add_u32 s49, s49, s47\n"
"s_mov_b32 s70, 344064\n"
"s_mov_b32 s71, 401408\n"
"s_mov_b32 s72, 458752\n"
"s_mov_b32 s73, 516096\n"
"s_mov_b32 s74, 573440\n"
"s_mov_b32 s75, 630784\n"
"s_mov_b32 s76, 688128\n"
"s_mov_b32 s77, 745472\n"
"s_mov_b32 s78, 802816\n"
"s_mov_b32 s79, 860160\n"
"s_mov_b32 s80, 917504\n"
"s_mov_b32 s81, 974848\n"
"s_mov_b32 s82, 1032192\n"
"s_mov_b32 s83, 1089536\n"
"s_mov_b32 s84, 1146880\n"
"s_mov_b32 s85, 1204224\n"
"s_mov_b32 s86, 1261568\n"
"s_mov_b32 s87, 1318912\n"
"s_mov_b32 s88, 1376256\n"
"s_mov_b32 s89, 1433600\n"
"s_mov_b32 s90, 1490944\n"
"s_mov_b32 s91, 1548288\n"
"s_mov_b32 s92, 1605632\n"
"s_mov_b32 s93, 1662976\n"
"s_mov_b32 s94, 1720320\n"
"s_mov_b32 s95, 1777664\n"
"s_lshr_b32 s52, s47, 2\n"
"v_mul_u32_u24 v5, 7168, v2\n"
"v_lshl_add_u32 v5, v3, 4, v5\n"
"v_add_u32 v57, s52, v5\n"
"s_and_b32 s52, s38, 1\n"
"s_mul_i32 s53, s45, 28672\n"
"s_lshl_b32 s54, s52, 10\n"
"s_add_u32 s54, s54, s47\n"
"s_add_u32 s54, s54, 2097152\n"
"s_mul_i32 s55, s52, 14336\n"
"s_add_u32 s55, s55, 4194304\n"
"s_bitcmp1_b32 s38, 1\n"
"s_cselect_b32 s54, s55, s54\n"
"s_cselect_b64 vcc, -1, 0\n"
"s_add_u32 s54, s54, s53\n"
"v_cndmask_b32 v5, v4, v57, vcc\n"
"v_add_u32 v10, s54, v5\n"
"v_subrev_u32 v11, 28672, v10\n"
"s_lshl_b32 s52, s45, 12\n"
"v_add_u32 v12, s52, v4\n"
"s_xor_b32 s53, s43, 1\n"
"s_mul_i32 s53, s53, 28\n"
"v_subrev_u32 v7, s53, v3\n"
"v_cmp_gt_u32 vcc, 4, v7\n"
"v_add_u32 v7, 2048, v12\n"
"v_add_u32 v8, 3072, v12\n"
"v_mov_b32 v5, 8192\n"
"v_cndmask_b32 v14, v7, v5, vcc\n"
"v_cndmask_b32 v16, v8, v5, vcc\n"
"v_add_u32 v13, 57456, v12\n"
"v_add_u32 v15, 57456, v14\n"
"v_add_u32 v17, 57456, v16\n"
"v_mov_b32 v9, 0xdddd\n"
"v_mov_b32 v76, 0\n"
"v_mov_b32 v77, 0\n"
"v_mov_b32 v78, 0\n"
"v_mov_b32 v79, 0\n"
"v_mov_b32 v80, 0\n"
"v_mov_b32 v81, 0\n"
"v_mov_b32 v82, 0\n"
"v_mov_b32 v83, 0\n"
"v_mov_b32 v84, 0\n"
"v_mov_b32 v85, 0\n"
"v_mov_b32 v86, 0\n"
"v_mov_b32 v87, 0\n"
"v_mov_b32 v88, 0\n"
"v_mov_b32 v89, 0\n"
"v_mov_b32 v90, 0\n"
"v_mov_b32 v91, 0\n"
"s_lshl_b32 s52, s46, 12\n"
"s_lshl_b32 s53, s45, 11\n"
"s_add_u32 s52, s52, 82080\n"
"s_add_u32 s54, s52, s53\n"
"v_add_u32 v28, s54, v4\n"
"s_xor_b32 s53, s53, 2048\n"
"s_add_u32 s54, s52, s53\n"
"v_add_u32 v29, s54, v4\n"
"s_lshl_b32 s52, s46, 9\n"
"s_add_u32 s52, s52, 159904\n"
"v_lshl_add_u32 v31, v3, 2, s52\n"
"s_lshl_b32 s53, s45, 8\n"
"s_xor_b32 s54, s43, 1\n"
"v_xor_b32 v5, s54, v2\n"
"v_lshl_add_u32 v5, v5, 7, v31\n"
"v_add_u32 v30, s53, v5\n"
"v_lshrrev_b32 v5, 4, v1\n"
"s_lshl_b32 s52, s38, 2\n"
"v_add_u32 v6, s52, v5\n"
"v_mul_u32_u24 v6, 896, v6\n"
"s_and_b32 s52, s38, 2\n"
"s_lshl_b32 s52, s52, 2\n"
"v_lshl_add_u32 v7, v5, 1, s52\n"
"v_and_b32 v8, 15, v1\n"
"v_xor_b32 v7, v7, v8\n"
"v_lshl_add_u32 v6, v7, 4, v6\n"
"s_and_b32 s53, s40, 3\n"
"s_lshl_b32 s54, s53, 8\n"
"v_add_u32 v6, s54, v6\n"
"s_cmp_eq_u32 s53, 3\n"
"s_cselect_b32 s52, 8, 16\n"
"v_cmp_gt_u32 vcc, s52, v7\n"
"v_mov_b32 v7, 2130706432\n"
"s_lshl_b32 s55, s38, 5\n"
"v_cndmask_b32 v18, v7, v6, vcc\n"
"s_add_u32 s54, s54, s55\n"
"s_cmp_lt_u32 s54, 896\n"
"s_cselect_b32 s54, s54, 2130706432\n"
"s_lshl_b32 s52, s39, 3\n"
"s_lshr_b32 s55, s40, 2\n"
"s_add_u32 s52, s52, s55\n"
"s_mul_i32 s55, s52, 57344\n"
"s_add_u32 s54, s54, s55\n"
"s_add_u32 s54, s54, 896\n"
"v_mul_u32_u24 v6, 1792, v3\n"
"v_lshl_add_u32 v6, v2, 4, v6\n"
"v_add_u32 v58, s54, v6\n"
"v_bfe_u32 v7, v1, 2, 2\n"
"v_lshl_add_u32 v5, v5, 3, v7\n"
"v_lshlrev_b32 v5, 8, v5\n"
"v_bfe_u32 v7, v1, 2, 3\n"
"v_xor_b32 v7, s38, v7\n"
"v_lshl_add_u32 v5, v7, 5, v5\n"
"v_and_b32 v7, 3, v1\n"
"v_lshl_add_u32 v5, v7, 3, v5\n"
"v_add_u32 v20, 82080, v5\n"
"v_add_u32 v21, 131232, v5\n"
"s_min_u32 s53, s40, 27\n"
"s_mul_i32 s54, s53, 37\n"
"s_lshr_b32 s54, s54, 8\n"
"s_mul_i32 s55, s54, 7\n"
"s_sub_u32 s53, s53, s55\n"
"s_lshl_b32 s55, s39, 2\n"
"s_add_u32 s54, s54, s55\n"
"s_lshl_b32 s53, s53, 7\n"
"v_lshrrev_b32 v5, 3, v1\n"
"s_lshl_b32 s55, s38, 3\n"
"v_add_u32 v5, s55, v5\n"
"v_mul_u32_u24 v5, 896, v5\n"
"v_bfe_u32 v7, v1, 4, 1\n"
"v_lshlrev_b32 v7, 2, v7\n"
"v_and_b32 v8, 7, v1\n"
"v_xor_b32 v7, v7, v8\n"
"v_lshl_add_u32 v5, v7, 4, v5\n"
"v_add_u32 v19, s53, v5\n"
"s_lshl_b32 s55, s54, 1\n"
"s_and_b32 s50, s38, 1\n"
"s_add_u32 s55, s55, s50\n"
"s_mul_i32 s55, s55, 57344\n"
"s_add_u32 s55, s55, s53\n"
"s_and_b32 s50, s38, 2\n"
"s_lshl_b32 s50, s50, 5\n"
"s_add_u32 s55, s55, s50\n"
"v_add_u32 v59, s55, v6\n"
"s_lshl_b32 s61, s52, 8\n"
"s_add_u32 s61, s61, s54\n"
"s_lshr_b32 s50, s47, 11\n"
"s_add_u32 s51, s47, 1536\n"
"s_lshr_b32 s51, s51, 11\n"
"s_sub_u32 s51, s51, s50\n"
"v_mul_u32_u24 v5, s51, v2\n"
"v_add_u32 v5, s50, v5\n"
"s_lshl_b32 s50, s44, 2\n"
"v_min_u32 v6, 3, v3\n"
"v_add_u32 v6, s50, v6\n"
"v_mul_u32_u24 v6, 43, v6\n"
"v_lshrrev_b32 v6, 7, v6\n"
"v_min_u32 v7, 21, v3\n"
"s_add_u32 s53, s46, 1\n"
"s_mul_i32 s53, s53, 14\n"
"s_cmp_lt_u32 s46, 2\n"
"s_cselect_b32 s53, s53, 0\n"
"s_cselect_b64 vcc, -1, 0\n"
"v_cndmask_b32 v6, v6, v7, vcc\n"
"v_add_u32 v7, s53, v5\n"
"v_lshrrev_b32 v7, 2, v7\n"
"v_lshl_add_u32 v6, v7, 5, v6\n"
"v_lshlrev_b32 v6, 7, v6\n"
"v_add_u32 v22, 16842752, v6\n"
"v_min_u32 v6, 4, v3\n"
"v_add_u32 v6, s50, v6\n"
"v_max_u32 v6, 1, v6\n"
"v_subrev_u32 v6, 1, v6\n"
"v_mul_u32_u24 v6, 43, v6\n"
"v_lshrrev_b32 v6, 7, v6\n"
"s_mul_i32 s53, s46, 14\n"
"v_add_u32 v7, s53, v5\n"
"v_lshrrev_b32 v7, 2, v7\n"
"v_lshl_add_u32 v6, v7, 5, v6\n"
"v_lshlrev_b32 v6, 7, v6\n"
"v_add_u32 v6, 16908288, v6\n"
"v_min_u32 v7, 13, v1\n"
"v_lshlrev_b32 v7, 7, v7\n"
"v_add_u32 v7, 16973824, v7\n"
"s_cmp_eq_u32 s46, 3\n"
"s_cselect_b64 vcc, -1, 0\n"
"v_cndmask_b32 v23, v6, v7, vcc\n"
"s_add_u32 s52, s50, s46\n"
"v_mov_b32 v220, 0x5555\n"
"v_mov_b32 v221, 0\n"
"v_add_u32 v6, 18, v3\n"
"v_lshlrev_b64 v[220:221], v6, v[220:221]\n"
"s_cmp_eq_u32 s52, 0\n"
"s_cselect_b32 s53, 0, -1\n"
"v_and_b32 v220, s53, v220\n"
"v_lshlrev_b32 v6, 2, v2\n"
"v_lshrrev_b32 v220, v6, v220\n"
"v_lshrrev_b32 v221, v6, v221\n"
"v_not_b32 v220, v220\n"
"v_not_b32 v221, v221\n"
"v_bfe_i32 v196, v221, 0, 1\n"
"v_and_b32 v196, 0xff800000, v196\n"
"v_bfe_i32 v197, v221, 1, 1\n"
"v_and_b32 v197, 0xff800000, v197\n"
"v_bfe_i32 v198, v221, 2, 1\n"
"v_and_b32 v198, 0xff800000, v198\n"
"v_bfe_i32 v199, v221, 3, 1\n"
"v_and_b32 v199, 0xff800000, v199\n"
"v_bfe_i32 v200, v221, 8, 1\n"
"v_and_b32 v200, 0xff800000, v200\n"
"v_bfe_i32 v201, v221, 9, 1\n"
"v_and_b32 v201, 0xff800000, v201\n"
"v_bfe_i32 v202, v221, 10, 1\n"
"v_and_b32 v202, 0xff800000, v202\n"
"v_bfe_i32 v203, v221, 11, 1\n"
"v_and_b32 v203, 0xff800000, v203\n"
"v_bfe_i32 v204, v221, 16, 1\n"
"v_and_b32 v204, 0xff800000, v204\n"
"v_bfe_i32 v205, v221, 17, 1\n"
"v_and_b32 v205, 0xff800000, v205\n"
"v_bfe_i32 v206, v221, 18, 1\n"
"v_and_b32 v206, 0xff800000, v206\n"
"v_bfe_i32 v207, v221, 19, 1\n"
"v_and_b32 v207, 0xff800000, v207\n"
"v_bfe_i32 v208, v221, 24, 1\n"
"v_and_b32 v208, 0xff800000, v208\n"
"v_bfe_i32 v209, v221, 25, 1\n"
"v_and_b32 v209, 0xff800000, v209\n"
"v_bfe_i32 v210, v221, 26, 1\n"
"v_and_b32 v210, 0xff800000, v210\n"
"v_bfe_i32 v211, v221, 27, 1\n"
"v_and_b32 v211, 0xff800000, v211\n"
"v_bfe_i32 v212, v220, 16, 1\n"
"v_and_b32 v212, 0xff800000, v212\n"
"v_bfe_i32 v213, v220, 17, 1\n"
"v_and_b32 v213, 0xff800000, v213\n"
"v_bfe_i32 v214, v220, 18, 1\n"
"v_and_b32 v214, 0xff800000, v214\n"
"v_bfe_i32 v215, v220, 19, 1\n"
"v_and_b32 v215, 0xff800000, v215\n"
"v_bfe_i32 v216, v220, 24, 1\n"
"v_and_b32 v216, 0xff800000, v216\n"
"v_bfe_i32 v217, v220, 25, 1\n"
"v_and_b32 v217, 0xff800000, v217\n"
"v_bfe_i32 v218, v220, 26, 1\n"
"v_and_b32 v218, 0xff800000, v218\n"
"v_bfe_i32 v219, v220, 27, 1\n"
"v_and_b32 v219, 0xff800000, v219\n"
"s_waitcnt vmcnt(0)\n"
"v_mov_b32 v244, 0x3f803f80\n"
"v_mov_b32 v245, 0x3f803f80\n"
"v_mov_b32 v246, 0x3f803f80\n"
"v_mov_b32 v247, 0x3f803f80\n"
"v_mov_b32 v248, 0x3f803f80\n"
"v_mov_b32 v249, 0x3f803f80\n"
"v_mov_b32 v250, 0x3f803f80\n"
"v_mov_b32 v251, 0x3f803f80\n"
"v_mov_b32 v252, 0x3f803f80\n"
"v_mov_b32 v253, 0x3f803f80\n"
"v_mov_b32 v254, 0x3f803f80\n"
"v_mov_b32 v255, 0x3f803f80\n"
"s_waitcnt lgkmcnt(0)\n"
"s_add_u32 s36, s36, 1\n"
"s_addc_u32 s37, s37, 0\n"
"s_waitcnt vmcnt(0)\n"
"s_barrier\n"
"s_lshl_b32 s50, s2, 7\n"
"s_add_u32 s50, s50, 17039360\n"
"s_cmp_eq_u32 s38, 4\n"
"s_cselect_b32 s50, s50, 2130706432\n"
"v_mov_b32 v24, s36\n"
"v_mov_b32 v25, s37\n"
"s_mov_b64 exec, 1\n"
"buffer_store_dwordx2 v[24:25], off, s[20:23], s50 sc1\n"
"s_mov_b64 exec, -1\n"
"s_lshl_b32 s50, s58, 7\n"
"s_add_u32 s50, s50, 16973824\n"
"s_cmp_eq_u32 s38, 4\n"
"s_cselect_b32 s50, s50, 2130706432\n"
"v_mov_b32 v24, s36\n"
"v_mov_b32 v25, s37\n"
"s_mov_b64 exec, 1\n"
"buffer_store_dwordx2 v[24:25], off, s[20:23], s50 sc1\n"
"s_mov_b64 exec, -1\n"
"s_mov_b32 s59, 2130706432\n"
"s_mov_b32 s58, 2130706432\n"
"s_cmp_lt_u32 s38, 4\n"
"s_cbranch_scc1 .Lfused_fwd_idle_c\n"
".Lfused_fwd_idle_l:\n"
"buffer_load_dwordx2 v[26:27], v22, s[20:23], 0 offen sc1\n"
"s_waitcnt vmcnt(0)\n"
"v_cmp_ne_u64 vcc, s[36:37], v[26:27]\n"
"s_cbranch_vccnz .Lfused_fwd_spin_i\n"
".Lfused_fwd_spin_i_r:\n"
"s_barrier\n"
"s_lshl_b32 s50, s46, 11\n"
"s_add_u32 m0, s50, 82080\n"
"s_add_u32 s51, s49, 0\n"
"buffer_load_dwordx4 v4, s[20:23], s51 offen sc1 lds\n"
"buffer_load_dwordx4 v4, s[20:23], s51 offen offset:1024 sc1 lds\n"
"s_add_u32 m0, s48, 61568\n"
"s_mov_b32 s52, 0\n"
"buffer_load_dwordx4 v11, s[20:23], s52 offen sc1 lds\n"
"s_add_u32 m0, s48, 65664\n"
"s_mov_b32 s52, 0\n"
"buffer_load_dwordx4 v10, s[20:23], s52 offen sc1 lds\n"
"s_add_u32 m0, s48, 69776\n"
"s_mov_b32 s52, 57344\n"
"buffer_load_dwordx4 v11, s[20:23], s52 offen sc1 lds\n"
"s_add_u32 m0, s48, 73872\n"
"s_mov_b32 s52, 57344\n"
"buffer_load_dwordx4 v10, s[20:23], s52 offen sc1 lds\n"
"s_add_u32 m0, s48, -4096\n"
"s_mov_b32 s52, 114688\n"
"buffer_load_dwordx4 v11, s[20:23], s52 offen sc1 lds\n"
"s_add_u32 m0, s48, 0\n"
"s_mov_b32 s52, 114688\n"
"buffer_load_dwordx4 v10, s[20:23], s52 offen sc1 lds\n"
"s_add_u32 m0, s48, 4112\n"
"s_mov_b32 s52, 172032\n"
"buffer_load_dwordx4 v11, s[20:23], s52 offen sc1 lds\n"
"s_add_u32 m0, s48, 8208\n"
"s_mov_b32 s52, 172032\n"
"buffer_load_dwordx4 v10, s[20:23], s52 offen sc1 lds\n"
"s_add_u32 m0, s48, 12320\n"
"s_mov_b32 s52, 229376\n"
"buffer_load_dwordx4 v11, s[20:23], s52 offen sc1 lds\n"
"s_add_u32 m0, s48, 16416\n"
"s_mov_b32 s52, 229376\n"
"buffer_load_dwordx4 v10, s[20:23], s52 offen sc1 lds\n"
"s_add_u32 m0, s48, 20528\n"
"s_mov_b32 s52, 286720\n"
"buffer_load_dwordx4 v11, s[20:23], s52 offen sc1 lds\n"
"s_add_u32 m0, s48, 24624\n"
"s_mov_b32 s52, 286720\n"
"buffer_load_dwordx4 v10, s[20:23], s52 offen sc1 lds\n"
"s_branch .Lfused_fwd_pro_l\n"
".Lfused_fwd_idle_c:\n"
"s_lshl_b32 s50, s38, 8\n"
"s_add_u32 s50, s50, s2\n"
"s_cmp_eq_u32 s50, 255\n"
"s_cbranch_scc0 .Lfused_fwd_idle_c1\n"
"v_lshlrev_b32 v5, 7, v1\n"
"s_mov_b32 s50, 200000\n"
".Lfused_fwd_epoll:\n"
"s_mov_b32 s51, 17039360\n"
"buffer_load_dwordx2 v[96:97], v5, s[20:23], s51 offen sc1\n"
"s_mov_b32 s51, 17047552\n"
"buffer_load_dwordx2 v[98:99], v5, s[20:23], s51 offen sc1\n"
"s_mov_b32 s51, 17055744\n"
"buffer_load_dwordx2 v[100:101], v5, s[20:23], s51 offen sc1\n"
"s_mov_b32 s51, 17063936\n"
"buffer_load_dwordx2 v[102:103], v5, s[20:23], s51 offen sc1\n"
"s_waitcnt vmcnt(0)\n"
"v_cmp_ne_u64 vcc, s[36:37], v[96:97]\n"
"v_cmp_ne_u64 s[52:53], s[36:37], v[98:99]\n"
"s_or_b64 vcc, vcc, s[52:53]\n"
"v_cmp_ne_u64 s[52:53], s[36:37], v[100:101]\n"
"s_or_b64 vcc, vcc, s[52:53]\n"
"v_cmp_ne_u64 s[52:53], s[36:37], v[102:103]\n"
"s_or_b64 vcc, vcc, s[52:53]\n"
"s_cbranch_vccz .Lfused_fwd_epoll_ok\n"
"s_sleep 8\n"
"s_sub_u32 s50, s50, 1\n"
"s_cmp_lg_u32 s50, 0\n"
"s_cbranch_scc1 .Lfused_fwd_epoll\n"
".Lfused_fwd_epoll_ok:\n"
"s_mov_b32 s50, 16777216\n"
"v_mov_b32 v24, s36\n"
"v_mov_b32 v25, s37\n"
"s_mov_b64 exec, 1\n"
"buffer_store_dwordx2 v[24:25], off, s[20:23], s50 sc1\n"
"s_mov_b64 exec, -1\n"
".Lfused_fwd_idle_c1:\n"
"v_mov_b32 v206, 0\n"
"v_mov_b32 v207, 0\n"
"s_barrier\n"
"s_branch .Lfused_fwd_pro_c\n"
".Lfused_fwd_pro_l:\n"
"s_cmp_eq_u32 s43, 0\n"
"s_cselect_b32 s4, -1, 0\n"
"s_mov_b32 s5, -1\n"
"s_mov_b32 s6, -1\n"
"s_cselect_b32 s7, 0, -1\n"
"s_cselect_b32 s8, 0x0fffffff, -16\n"
"s_mov_b32 s9, s8\n"
"s_and_b32 s10, s61, 0xff\n"
"s_mul_i32 s10, s10, 57344\n"
"s_add_u32 s10, s10, 12582912\n"
"s_lshr_b32 s11, s61, 8\n"
"s_mul_i32 s11, s11, 28672\n"
"s_add_u32 s11, s11, 14680064\n"
"s_cmp_eq_u32 s43, 0\n"
"s_cselect_b32 s50, 1024, 8\n"
"s_and_b64 vcc, s[4:5], s[6:7]\n"
"v_mov_b32 v5, 2130706432\n"
"v_add_u32 v6, s50, v4\n"
"v_cndmask_b32 v56, v5, v4, s[4:5]\n"
"v_cndmask_b32 v22, v5, v4, s[6:7]\n"
"v_cndmask_b32 v11, v6, v5, vcc\n"
"v_mul_u32_u24 v7, 8208, v1\n"
"v_min_u32 v7, 73872, v7\n"
"ds_write_b128 v7, v[244:247] offset:8192\n"
"s_lshl_b32 s50, s46, 11\n"
"s_add_u32 s50, s50, 82080\n"
"v_add_u32 v5, s50, v4\n"
"v_add_u32 v6, 65664, v4\n"
"s_waitcnt vmcnt(8) lgkmcnt(0)\n"
"s_barrier\n"
"ds_read_b128 v[32:35], v5\n"
"ds_read_b128 v[36:39], v5 offset:1024\n"
"ds_read_b128 v[48:51], v6\n"
"ds_read_b128 v[52:55], v6 offset:1024\n"
"ds_read_b128 v[40:43], v13 offset:8208\n"
"ds_read_b128 v[44:47], v13 offset:9232\n"
"s_waitcnt lgkmcnt(4)\n"
"v_cndmask_b32 v32, 0, v32, s[4:5]\n"
"v_cndmask_b32 v33, 0, v33, s[4:5]\n"
"v_cndmask_b32 v38, 0, v38, s[6:7]\n"
"v_cndmask_b32 v39, 0, v39, s[6:7]\n"
"s_waitcnt lgkmcnt(2)\n"
"v_mfma_f32_32x32x16_bf16 v[108:123], v[48:51], v[32:35], 0\n"
"v_mfma_f32_32x32x16_bf16 v[108:123], v[52:55], v[36:39], v[108:123]\n"
"ds_read_b128 v[48:51], v15 offset:8208\n"
"s_nop 10\n"
"v_max3_f32 v7, v108, v109, v110\n"
"v_max3_f32 v8, v111, v112, v113\n"
"v_max3_f32 v7, v7, v114, v115\n"
"v_max3_f32 v8, v8, v116, v117\n"
"v_max3_f32 v7, v7, v118, v119\n"
"v_max3_f32 v8, v8, v120, v121\n"
"v_max3_f32 v7, v7, v122, v123\n"
"v_max_f32 v7, v7, v8\n"
"v_mov_b32 v8, v7\n"
"s_nop 1\n"
"v_permlane32_swap_b32 v7, v8\n"
"v_max_f32 v7, v7, v8\n"
"v_sub_f32 v60, 0xc2c00000, v7\n"
"v_mov_b32 v61, v60\n"
"v_mov_b32 v62, v60\n"
"v_mov_b32 v63, v60\n"
"v_mov_b32 v64, v60\n"
"v_mov_b32 v65, v60\n"
"v_mov_b32 v66, v60\n"
"v_mov_b32 v67, v60\n"
"v_mov_b32 v68, v60\n"
"v_mov_b32 v69, v60\n"
"v_mov_b32 v70, v60\n"
"v_mov_b32 v71, v60\n"
"v_mov_b32 v72, v60\n"
"v_mov_b32 v73, v60\n"
"v_mov_b32 v74, v60\n"
"v_mov_b32 v75, v60\n"
"s_waitcnt lgkmcnt(0)\n"
"s_setprio 3\n"
"v_mfma_f32_32x32x16_bf16 v[92:107], v[40:43], v[32:35], v[60:75]\n"
"ds_read_b128 v[40:43], v13 offset:16416\n"
"v_mfma_f32_32x32x16_bf16 v[92:107], v[44:47], v[36:39], v[92:107]\n"
"ds_read_b128 v[44:47], v13 offset:17440\n"
"s_waitcnt vmcnt(4) lgkmcnt(0)\n"
"s_barrier\n"
"s_nop 7\n"
"v_mfma_f32_32x32x16_bf16 v[108:123], v[40:43], v[32:35], v[60:75]\n"
"ds_read_b128 v[40:43], v12 offset:0\n"
"v_exp_f32 v92, v92\n"
"v_exp_f32 v93, v93\n"
"v_exp_f32 v94, v94\n"
"v_exp_f32 v95, v95\n"
"s_add_u32 m0, s48, 32832\n"
"ds_read_b128 v[52:55], v17 offset:8208\n"
"buffer_load_dwordx4 v10, s[20:23], s70 offen sc1 lds\n"
"s_setprio 2\n"
"v_exp_f32 v96, v96\n"
"v_exp_f32 v97, v97\n"
"v_exp_f32 v98, v98\n"
"v_exp_f32 v99, v99\n"
"v_mfma_f32_32x32x16_bf16 v[108:123], v[44:47], v[36:39], v[108:123]\n"
"s_add_u32 m0, s48, 41040\n"
"ds_read_b128 v[44:47], v12 offset:1024\n"
"buffer_load_dwordx4 v10, s[20:23], s71 offen sc1 lds\n"
"v_exp_f32 v100, v100\n"
"v_exp_f32 v101, v101\n"
"v_exp_f32 v102, v102\n"
"v_exp_f32 v103, v103\n"
"v_smfmac_f32_32x32x32_bf16 v[76:91], v[48:51], v[92:99], v9\n"
"ds_read_b128 v[48:51], v15 offset:16416\n"
"s_waitcnt lgkmcnt(2)\n"
"s_setprio 1\n"
"v_exp_f32 v104, v104\n"
"v_exp_f32 v105, v105\n"
"v_exp_f32 v106, v106\n"
"v_exp_f32 v107, v107\n"
"v_mfma_f32_32x32x16_bf16 v[124:139], v[40:43], v[32:35], v[60:75]\n"
"ds_read_b128 v[40:43], v12 offset:8208\n"
"v_exp_f32 v108, v108\n"
"v_exp_f32 v109, v109\n"
"v_exp_f32 v110, v110\n"
"v_exp_f32 v111, v111\n"
"v_smfmac_f32_32x32x32_bf16 v[76:91], v[52:55], v[100:107], v9\n"
"s_add_u32 m0, s48, 49248\n"
"ds_read_b128 v[52:55], v17 offset:16416\n"
"buffer_load_dwordx4 v10, s[20:23], s72 offen sc1 lds\n"
"s_waitcnt lgkmcnt(2)\n"
"s_setprio 0\n"
"v_exp_f32 v112, v112\n"
"v_exp_f32 v113, v113\n"
"v_exp_f32 v114, v114\n"
"v_exp_f32 v115, v115\n"
"v_mfma_f32_32x32x16_bf16 v[124:139], v[44:47], v[36:39], v[124:139]\n"
"s_add_u32 m0, s48, 57456\n"
"ds_read_b128 v[44:47], v12 offset:9232\n"
"buffer_load_dwordx4 v10, s[20:23], s73 offen sc1 lds\n"
"v_exp_f32 v116, v116\n"
"v_exp_f32 v117, v117\n"
"v_exp_f32 v118, v118\n"
"v_exp_f32 v119, v119\n"
"v_smfmac_f32_32x32x32_bf16 v[76:91], v[48:51], v[108:115], v9\n"
"ds_read_b128 v[48:51], v14 offset:0\n"
"s_waitcnt vmcnt(4) lgkmcnt(2)\n"
"s_barrier\n"
"s_setprio 3\n"
"v_exp_f32 v120, v120\n"
"v_exp_f32 v121, v121\n"
"v_exp_f32 v122, v122\n"
"v_exp_f32 v123, v123\n"
"v_mfma_f32_32x32x16_bf16 v[92:107], v[40:43], v[32:35], v[60:75]\n"
"ds_read_b128 v[40:43], v12 offset:16416\n"
"v_exp_f32 v124, v124\n"
"v_exp_f32 v125, v125\n"
"v_exp_f32 v126, v126\n"
"v_exp_f32 v127, v127\n"
"v_smfmac_f32_32x32x32_bf16 v[76:91], v[52:55], v[116:123], v9\n"
"s_add_u32 m0, s48, 65664\n"
"ds_read_b128 v[52:55], v16 offset:0\n"
"buffer_load_dwordx4 v10, s[20:23], s74 offen sc1 lds\n"
"s_waitcnt lgkmcnt(2)\n"
"s_setprio 2\n"
"v_exp_f32 v128, v128\n"
"v_exp_f32 v129, v129\n"
"v_exp_f32 v130, v130\n"
"v_exp_f32 v131, v131\n"
"v_mfma_f32_32x32x16_bf16 v[92:107], v[44:47], v[36:39], v[92:107]\n"
"ds_read_b128 v[44:47], v12 offset:17440\n"
"v_exp_f32 v132, v132\n"
"v_exp_f32 v133, v133\n"
"v_exp_f32 v134, v134\n"
"v_exp_f32 v135, v135\n"
"v_smfmac_f32_32x32x32_bf16 v[76:91], v[48:51], v[124:131], v9\n"
"ds_read_b128 v[48:51], v14 offset:8208\n"
"s_waitcnt lgkmcnt(2)\n"
"s_setprio 1\n"
"v_exp_f32 v136, v136\n"
"v_exp_f32 v137, v137\n"
"v_exp_f32 v138, v138\n"
"v_exp_f32 v139, v139\n"
"v_mfma_f32_32x32x16_bf16 v[108:123], v[40:43], v[32:35], v[60:75]\n"
"ds_read_b128 v[40:43], v12 offset:24624\n"
"v_exp_f32 v92, v92\n"
"v_exp_f32 v93, v93\n"
"v_exp_f32 v94, v94\n"
"v_exp_f32 v95, v95\n"
"v_smfmac_f32_32x32x32_bf16 v[76:91], v[52:55], v[132:139], v9\n"
"s_add_u32 m0, s48, 73872\n"
"ds_read_b128 v[52:55], v16 offset:8208\n"
"buffer_load_dwordx4 v10, s[20:23], s75 offen sc1 lds\n"
"s_waitcnt lgkmcnt(2)\n"
"s_setprio 0\n"
"v_exp_f32 v96, v96\n"
"v_exp_f32 v97, v97\n"
"v_exp_f32 v98, v98\n"
"v_exp_f32 v99, v99\n"
"v_mfma_f32_32x32x16_bf16 v[108:123], v[44:47], v[36:39], v[108:123]\n"
"ds_read_b128 v[44:47], v12 offset:25648\n"
"v_exp_f32 v100, v100\n"
"v_exp_f32 v101, v101\n"
"v_exp_f32 v102, v102\n"
"v_exp_f32 v103, v103\n"
"v_smfmac_f32_32x32x32_bf16 v[76:91], v[48:51], v[92:99], v9\n"
"ds_read_b128 v[48:51], v14 offset:16416\n"
"s_waitcnt vmcnt(4) lgkmcnt(2)\n"
"s_barrier\n"
"s_setprio 3\n"
"v_exp_f32 v104, v104\n"
"v_exp_f32 v105, v105\n"
"v_exp_f32 v106, v106\n"
"v_exp_f32 v107, v107\n"
"v_mfma_f32_32x32x16_bf16 v[124:139], v[40:43], v[32:35], v[60:75]\n"
"ds_read_b128 v[40:43], v12 offset:32832\n"
"v_exp_f32 v108, v108\n"
"v_exp_f32 v109, v109\n"
"v_exp_f32 v110, v110\n"
"v_exp_f32 v111, v111\n"
"v_smfmac_f32_32x32x32_bf16 v[76:91], v[52:55], v[100:107], v9\n"
"s_add_u32 m0, s48, 0\n"
"ds_read_b128 v[52:55], v16 offset:16416\n"
"buffer_load_dwordx4 v10, s[20:23], s76 offen sc1 lds\n"
"s_waitcnt lgkmcnt(2)\n"
"s_setprio 2\n"
"v_exp_f32 v112, v112\n"
"v_exp_f32 v113, v113\n"
"v_exp_f32 v114, v114\n"
"v_exp_f32 v115, v115\n"
"v_mfma_f32_32x32x16_bf16 v[124:139], v[44:47], v[36:39], v[124:139]\n"
"ds_read_b128 v[44:47], v12 offset:33856\n"
"v_exp_f32 v116, v116\n"
"v_exp_f32 v117, v117\n"
"v_exp_f32 v118, v118\n"
"v_exp_f32 v119, v119\n"
"v_smfmac_f32_32x32x32_bf16 v[76:91], v[48:51], v[108:115], v9\n"
"ds_read_b128 v[48:51], v14 offset:24624\n"
"s_waitcnt lgkmcnt(2)\n"
"s_setprio 1\n"
"v_exp_f32 v120, v120\n"
"v_exp_f32 v121, v121\n"
"v_exp_f32 v122, v122\n"
"v_exp_f32 v123, v123\n"
"v_mfma_f32_32x32x16_bf16 v[92:107], v[40:43], v[32:35], v[60:75]\n"
"ds_read_b128 v[40:43], v12 offset:41040\n"
"v_exp_f32 v124, v124\n"
"v_exp_f32 v125, v125\n"
"v_exp_f32 v126, v126\n"
"v_exp_f32 v127, v127\n"
"v_smfmac_f32_32x32x32_bf16 v[76:91], v[52:55], v[116:123], v9\n"
"s_add_u32 m0, s48, 8208\n"
"ds_read_b128 v[52:55], v16 offset:24624\n"
"buffer_load_dwordx4 v10, s[20:23], s77 offen sc1 lds\n"
"s_waitcnt lgkmcnt(2)\n"
"s_setprio 0\n"
"v_exp_f32 v128, v128\n"
"v_exp_f32 v129, v129\n"
"v_exp_f32 v130, v130\n"
"v_exp_f32 v131, v131\n"
"v_mfma_f32_32x32x16_bf16 v[92:107], v[44:47], v[36:39], v[92:107]\n"
"ds_read_b128 v[44:47], v12 offset:42064\n"
"v_exp_f32 v132, v132\n"
"v_exp_f32 v133, v133\n"
"v_exp_f32 v134, v134\n"
"v_exp_f32 v135, v135\n"
"v_smfmac_f32_32x32x32_bf16 v[76:91], v[48:51], v[124:131], v9\n"
"ds_read_b128 v[48:51], v14 offset:32832\n"
"s_waitcnt vmcnt(4) lgkmcnt(2)\n"
"s_barrier\n"
"s_setprio 3\n"
"v_exp_f32 v136, v136\n"
"v_exp_f32 v137, v137\n"
"v_exp_f32 v138, v138\n"
"v_exp_f32 v139, v139\n"
"v_mfma_f32_32x32x16_bf16 v[108:123], v[40:43], v[32:35], v[60:75]\n"
"ds_read_b128 v[40:43], v12 offset:49248\n"
"v_exp_f32 v92, v92\n"
"v_exp_f32 v93, v93\n"
"v_exp_f32 v94, v94\n"
"v_exp_f32 v95, v95\n"
"v_smfmac_f32_32x32x32_bf16 v[76:91], v[52:55], v[132:139], v9\n"
"s_add_u32 m0, s48, 16416\n"
"ds_read_b128 v[52:55], v16 offset:32832\n"
"buffer_load_dwordx4 v10, s[20:23], s78 offen sc1 lds\n"
"s_waitcnt lgkmcnt(2)\n"
"s_setprio 2\n"
"v_exp_f32 v96, v96\n"
"v_exp_f32 v97, v97\n"
"v_exp_f32 v98, v98\n"
"v_exp_f32 v99, v99\n"
"v_mfma_f32_32x32x16_bf16 v[108:123], v[44:47], v[36:39], v[108:123]\n"
"s_add_u32 m0, s48, 147616\n"
"s_mov_b32 s17, 0\n"
"ds_read_b128 v[44:47], v12 offset:50272\n"
"buffer_load_dwordx4 v18, s[24:27], s17 offen lds\n"
"v_exp_f32 v100, v100\n"
"v_exp_f32 v101, v101\n"
"v_exp_f32 v102, v102\n"
"v_exp_f32 v103, v103\n"
"v_smfmac_f32_32x32x32_bf16 v[76:91], v[48:51], v[92:99], v9\n"
"ds_read_b128 v[48:51], v14 offset:41040\n"
"s_waitcnt lgkmcnt(2)\n"
"s_setprio 1\n"
"v_exp_f32 v104, v104\n"
"v_exp_f32 v105, v105\n"
"v_exp_f32 v106, v106\n"
"v_exp_f32 v107, v107\n"
"v_mfma_f32_32x32x16_bf16 v[124:139], v[40:43], v[32:35], v[60:75]\n"
"ds_read_b128 v[40:43], v13 offset:0\n"
"v_exp_f32 v108, v108\n"
"v_exp_f32 v109, v109\n"
"v_exp_f32 v110, v110\n"
"v_exp_f32 v111, v111\n"
"v_smfmac_f32_32x32x32_bf16 v[76:91], v[52:55], v[100:107], v9\n"
"s_add_u32 m0, s48, 24624\n"
"ds_read_b128 v[52:55], v16 offset:41040\n"
"buffer_load_dwordx4 v10, s[20:23], s79 offen sc1 lds\n"
"s_waitcnt lgkmcnt(2)\n"
"s_setprio 0\n"
"v_exp_f32 v112, v112\n"
"v_exp_f32 v113, v113\n"
"v_exp_f32 v114, v114\n"
"v_exp_f32 v115, v115\n"
"v_mfma_f32_32x32x16_bf16 v[124:139], v[44:47], v[36:39], v[124:139]\n"
"s_add_u32 m0, s48, 139424\n"
"s_mov_b32 s17, 28672\n"
"ds_read_b128 v[44:47], v13 offset:1024\n"
"buffer_load_dwordx4 v18, s[24:27], s17 offen lds\n"
"v_exp_f32 v116, v116\n"
"v_exp_f32 v117, v117\n"
"v_exp_f32 v118, v118\n"
"v_exp_f32 v119, v119\n"
"v_smfmac_f32_32x32x32_bf16 v[76:91], v[48:51], v[108:115], v9\n"
"ds_read_b128 v[48:51], v14 offset:49248\n"
"s_waitcnt vmcnt(6) lgkmcnt(2)\n"
"s_barrier\n"
"s_setprio 3\n"
"v_exp_f32 v120, v120\n"
"v_exp_f32 v121, v121\n"
"v_exp_f32 v122, v122\n"
"v_exp_f32 v123, v123\n"
"v_mfma_f32_32x32x16_bf16 v[92:107], v[40:43], v[32:35], v[60:75]\n"
"ds_read_b128 v[40:43], v13 offset:8208\n"
"v_exp_f32 v124, v124\n"
"v_exp_f32 v125, v125\n"
"v_exp_f32 v126, v126\n"
"v_exp_f32 v127, v127\n"
"v_smfmac_f32_32x32x32_bf16 v[76:91], v[52:55], v[116:123], v9\n"
"s_add_u32 m0, s48, 32832\n"
"ds_read_b128 v[52:55], v16 offset:49248\n"
"buffer_load_dwordx4 v10, s[20:23], s80 offen sc1 lds\n"
"s_waitcnt lgkmcnt(2)\n"
"s_setprio 2\n"
"v_exp_f32 v128, v128\n"
"v_exp_f32 v129, v129\n"
"v_exp_f32 v130, v130\n"
"v_exp_f32 v131, v131\n"
"v_mfma_f32_32x32x16_bf16 v[92:107], v[44:47], v[36:39], v[92:107]\n"
"s_add_u32 m0, s48, 131232\n"
"s_mov_b32 s17, 57344\n"
"ds_read_b128 v[44:47], v13 offset:9232\n"
"buffer_load_dwordx4 v18, s[24:27], s17 offen lds\n"
"v_exp_f32 v132, v132\n"
"v_exp_f32 v133, v133\n"
"v_exp_f32 v134, v134\n"
"v_exp_f32 v135, v135\n"
"v_smfmac_f32_32x32x32_bf16 v[76:91], v[48:51], v[124:131], v9\n"
"ds_read_b128 v[48:51], v15 offset:0\n"
"s_waitcnt lgkmcnt(2)\n"
"s_setprio 1\n"
"v_exp_f32 v136, v136\n"
"v_exp_f32 v137, v137\n"
"v_exp_f32 v138, v138\n"
"v_exp_f32 v139, v139\n"
"v_mfma_f32_32x32x16_bf16 v[108:123], v[40:43], v[32:35], v[60:75]\n"
"ds_read_b128 v[40:43], v13 offset:16416\n"
"v_exp_f32 v92, v92\n"
"v_exp_f32 v93, v93\n"
"v_exp_f32 v94, v94\n"
"v_exp_f32 v95, v95\n"
"v_smfmac_f32_32x32x32_bf16 v[76:91], v[52:55], v[132:139], v9\n"
"s_add_u32 m0, s48, 41040\n"
"ds_read_b128 v[52:55], v17 offset:0\n"
"buffer_load_dwordx4 v10, s[20:23], s81 offen sc1 lds\n"
"s_waitcnt lgkmcnt(2)\n"
"s_setprio 0\n"
"v_exp_f32 v96, v96\n"
"v_exp_f32 v97, v97\n"
"v_exp_f32 v98, v98\n"
"v_exp_f32 v99, v99\n"
"v_mfma_f32_32x32x16_bf16 v[108:123], v[44:47], v[36:39], v[108:123]\n"
"s_add_u32 m0, s48, 123040\n"
"s_mov_b32 s17, 86016\n"
"ds_read_b128 v[44:47], v13 offset:17440\n"
"buffer_load_dwordx4 v18, s[24:27], s17 offen lds\n"
"v_exp_f32 v100, v100\n"
"v_exp_f32 v101, v101\n"
"v_exp_f32 v102, v102\n"
"v_exp_f32 v103, v103\n"
"v_smfmac_f32_32x32x32_bf16 v[76:91], v[48:51], v[92:99], v9\n"
"ds_read_b128 v[48:51], v15 offset:8208\n"
"s_waitcnt vmcnt(4) lgkmcnt(2)\n"
"s_barrier\n"
"s_setprio 3\n"
"v_exp_f32 v104, v104\n"
"v_exp_f32 v105, v105\n"
"v_exp_f32 v106, v106\n"
"v_exp_f32 v107, v107\n"
"v_mfma_f32_32x32x16_bf16 v[124:139], v[40:43], v[32:35], v[60:75]\n"
"ds_read_b128 v[40:43], v12 offset:0\n"
"ds_read_b64_tr_b16 v[140:141], v21 offset:16384\n"
"v_exp_f32 v108, v108\n"
"v_exp_f32 v109, v109\n"
"v_exp_f32 v110, v110\n"
"v_exp_f32 v111, v111\n"
"v_smfmac_f32_32x32x32_bf16 v[76:91], v[52:55], v[100:107], v9\n"
"s_add_u32 m0, s48, 49248\n"
"ds_read_b128 v[52:55], v17 offset:8208\n"
"buffer_load_dwordx4 v10, s[20:23], s82 offen sc1 lds\n"
"ds_read_b64_tr_b16 v[142:143], v21 offset:17408\n"
"s_waitcnt lgkmcnt(4)\n"
"s_setprio 2\n"
"v_exp_f32 v112, v112\n"
"v_exp_f32 v113, v113\n"
"v_exp_f32 v114, v114\n"
"v_exp_f32 v115, v115\n"
"v_mfma_f32_32x32x16_bf16 v[124:139], v[44:47], v[36:39], v[124:139]\n"
"s_add_u32 m0, s48, 114848\n"
"s_mov_b32 s17, 114688\n"
"ds_read_b128 v[44:47], v12 offset:1024\n"
"buffer_load_dwordx4 v18, s[24:27], s17 offen lds\n"
"v_exp_f32 v116, v116\n"
"v_exp_f32 v117, v117\n"
"v_exp_f32 v118, v118\n"
"v_exp_f32 v119, v119\n"
"v_smfmac_f32_32x32x32_bf16 v[76:91], v[48:51], v[108:115], v9\n"
"ds_read_b128 v[48:51], v15 offset:16416\n"
"s_waitcnt lgkmcnt(3)\n"
"s_setprio 1\n"
"v_exp_f32 v120, v120\n"
"v_exp_f32 v121, v121\n"
"v_exp_f32 v122, v122\n"
"v_exp_f32 v123, v123\n"
"v_mfma_f32_32x32x16_bf16 v[92:107], v[40:43], v[32:35], v[60:75]\n"
"s_add_u32 m0, s48, 57456\n"
"ds_read_b128 v[40:43], v12 offset:8208\n"
"buffer_load_dwordx4 v10, s[20:23], s83 offen sc1 lds\n"
"ds_read_b64_tr_b16 v[144:145], v21 offset:8192\n"
"v_exp_f32 v124, v124\n"
"v_exp_f32 v125, v125\n"
"v_exp_f32 v126, v126\n"
"v_exp_f32 v127, v127\n"
"v_smfmac_f32_32x32x32_bf16 v[76:91], v[52:55], v[116:123], v9\n"
"ds_read_b128 v[52:55], v17 offset:16416\n"
"buffer_load_dwordx2 v[26:27], v23, s[20:23], 0 offen sc1\n"
"ds_read_b64_tr_b16 v[146:147], v21 offset:9216\n"
"s_waitcnt lgkmcnt(4)\n"
"s_setprio 0\n"
"v_exp_f32 v128, v128\n"
"v_exp_f32 v129, v129\n"
"v_exp_f32 v130, v130\n"
"v_exp_f32 v131, v131\n"
"v_mfma_f32_32x32x16_bf16 v[92:107], v[44:47], v[36:39], v[92:107]\n"
"s_add_u32 m0, s48, 106656\n"
"s_mov_b32 s17, 143360\n"
"ds_read_b128 v[44:47], v12 offset:9232\n"
"buffer_load_dwordx4 v18, s[24:27], s17 offen lds\n"
"v_exp_f32 v132, v132\n"
"v_exp_f32 v133, v133\n"
"v_exp_f32 v134, v134\n"
"v_exp_f32 v135, v135\n"
"v_smfmac_f32_32x32x32_bf16 v[76:91], v[48:51], v[124:131], v9\n"
"ds_read_b128 v[48:51], v14 offset:0\n"
"s_waitcnt vmcnt(5) lgkmcnt(2)\n"
"s_barrier\n"
"s_setprio 3\n"
"v_exp_f32 v136, v136\n"
"v_exp_f32 v137, v137\n"
"v_exp_f32 v138, v138\n"
"v_exp_f32 v139, v139\n"
"v_mfma_f32_32x32x16_bf16 v[108:123], v[40:43], v[32:35], v[60:75]\n"
"ds_read_b128 v[40:43], v12 offset:16416\n"
"ds_read_b64_tr_b16 v[148:149], v21 offset:0\n"
"v_exp_f32 v92, v92\n"
"v_exp_f32 v93, v93\n"
"v_exp_f32 v94, v94\n"
"v_exp_f32 v95, v95\n"
"v_smfmac_f32_32x32x32_bf16 v[76:91], v[52:55], v[132:139], v9\n"
"s_add_u32 m0, s48, 65664\n"
"ds_read_b128 v[52:55], v16 offset:0\n"
"buffer_load_dwordx4 v10, s[20:23], s84 offen sc1 lds\n"
"ds_read_b64_tr_b16 v[150:151], v21 offset:1024\n"
"s_waitcnt lgkmcnt(4)\n"
"s_setprio 2\n"
"v_exp_f32 v96, v96\n"
"v_exp_f32 v97, v97\n"
"v_exp_f32 v98, v98\n"
"v_exp_f32 v99, v99\n"
"v_mfma_f32_32x32x16_bf16 v[108:123], v[44:47], v[36:39], v[108:123]\n"
"s_add_u32 m0, s48, 147616\n"
"s_mov_b32 s17, 172032\n"
"ds_read_b128 v[44:47], v12 offset:17440\n"
"buffer_load_dwordx4 v18, s[24:27], s17 offen lds\n"
"v_exp_f32 v100, v100\n"
"v_exp_f32 v101, v101\n"
"v_exp_f32 v102, v102\n"
"v_exp_f32 v103, v103\n"
"v_smfmac_f32_32x32x32_bf16 v[76:91], v[48:51], v[92:99], v9\n"
"ds_read_b128 v[48:51], v14 offset:8208\n"
"s_waitcnt lgkmcnt(3)\n"
"s_setprio 1\n"
"v_exp_f32 v104, v104\n"
"v_exp_f32 v105, v105\n"
"v_exp_f32 v106, v106\n"
"v_exp_f32 v107, v107\n"
"v_mfma_f32_32x32x16_bf16 v[124:139], v[40:43], v[32:35], v[60:75]\n"
"ds_read_b128 v[40:43], v12 offset:24624\n"
"ds_read_b64_tr_b16 v[152:153], v20 offset:40960\n"
"v_exp_f32 v108, v108\n"
"v_exp_f32 v109, v109\n"
"v_exp_f32 v110, v110\n"
"v_exp_f32 v111, v111\n"
"v_smfmac_f32_32x32x32_bf16 v[76:91], v[52:55], v[100:107], v9\n"
"s_add_u32 m0, s48, 73872\n"
"ds_read_b128 v[52:55], v16 offset:8208\n"
"buffer_load_dwordx4 v10, s[20:23], s85 offen sc1 lds\n"
"ds_read_b64_tr_b16 v[154:155], v20 offset:41984\n"
"s_waitcnt lgkmcnt(4)\n"
"s_setprio 0\n"
"v_exp_f32 v112, v112\n"
"v_exp_f32 v113, v113\n"
"v_exp_f32 v114, v114\n"
"v_exp_f32 v115, v115\n"
"v_mfma_f32_32x32x16_bf16 v[124:139], v[44:47], v[36:39], v[124:139]\n"
"s_add_u32 m0, s48, 139424\n"
"s_mov_b32 s17, 200704\n"
"ds_read_b128 v[44:47], v12 offset:25648\n"
"buffer_load_dwordx4 v18, s[24:27], s17 offen lds\n"
"v_exp_f32 v116, v116\n"
"v_exp_f32 v117, v117\n"
"v_exp_f32 v118, v118\n"
"v_exp_f32 v119, v119\n"
"v_smfmac_f32_32x32x32_bf16 v[76:91], v[48:51], v[108:115], v9\n"
"ds_read_b128 v[48:51], v14 offset:16416\n"
"s_waitcnt vmcnt(4) lgkmcnt(2)\n"
"v_cmp_ne_u64 vcc, s[36:37], v[26:27]\n"
"s_cbranch_vccnz .Lfused_fwd_spin_a\n"
".Lfused_fwd_spin_a_r:\n"
"s_barrier\n"
"s_setprio 3\n"
"v_exp_f32 v120, v120\n"
"v_exp_f32 v121, v121\n"
"v_exp_f32 v122, v122\n"
"v_exp_f32 v123, v123\n"
"v_mfma_f32_32x32x16_bf16 v[92:107], v[40:43], v[32:35], v[60:75]\n"
"s_add_u32 m0, s48, 0\n"
"ds_read_b128 v[40:43], v12 offset:32832\n"
"buffer_load_dwordx4 v10, s[20:23], s86 offen sc1 lds\n"
"ds_read_b64_tr_b16 v[156:157], v20 offset:32768\n"
"v_exp_f32 v124, v124\n"
"v_exp_f32 v125, v125\n"
"v_exp_f32 v126, v126\n"
"v_exp_f32 v127, v127\n"
"v_smfmac_f32_32x32x32_bf16 v[76:91], v[52:55], v[116:123], v9\n"
"s_add_u32 s14, s49, 6291456\n"
"ds_read_b128 v[52:55], v16 offset:16416\n"
"buffer_load_dwordx4 v[220:223], v4, s[20:23], s14 offen sc1\n"
"ds_read_b64_tr_b16 v[158:159], v20 offset:33792\n"
"s_waitcnt lgkmcnt(4)\n"
"s_setprio 2\n"
"v_exp_f32 v128, v128\n"
"v_exp_f32 v129, v129\n"
"v_exp_f32 v130, v130\n"
"v_exp_f32 v131, v131\n"
"v_mfma_f32_32x32x16_bf16 v[92:107], v[44:47], v[36:39], v[92:107]\n"
"ds_read_b128 v[44:47], v12 offset:33856\n"
"buffer_load_dwordx4 v[224:227], v4, s[20:23], s14 offen offset:1024 sc1\n"
"v_exp_f32 v132, v132\n"
"v_exp_f32 v133, v133\n"
"v_exp_f32 v134, v134\n"
"v_exp_f32 v135, v135\n"
"v_smfmac_f32_32x32x32_bf16 v[76:91], v[48:51], v[124:131], v9\n"
"s_add_u32 s14, s49, 8388608\n"
"ds_read_b128 v[48:51], v14 offset:24624\n"
"buffer_load_dwordx4 v[228:231], v4, s[20:23], s14 offen sc1\n"
"s_waitcnt lgkmcnt(3)\n"
"s_setprio 1\n"
"v_exp_f32 v136, v136\n"
"v_exp_f32 v137, v137\n"
"v_exp_f32 v138, v138\n"
"v_exp_f32 v139, v139\n"
"v_mfma_f32_32x32x16_bf16 v[108:123], v[40:43], v[32:35], v[60:75]\n"
"s_add_u32 m0, s48, 8208\n"
"ds_read_b128 v[40:43], v12 offset:41040\n"
"buffer_load_dwordx4 v10, s[20:23], s87 offen sc1 lds\n"
"ds_read_b64_tr_b16 v[160:161], v20 offset:24576\n"
"v_exp_f32 v92, v92\n"
"v_exp_f32 v93, v93\n"
"v_exp_f32 v94, v94\n"
"v_exp_f32 v95, v95\n"
"v_smfmac_f32_32x32x32_bf16 v[76:91], v[52:55], v[132:139], v9\n"
"ds_read_b128 v[52:55], v16 offset:24624\n"
"buffer_load_dwordx4 v[232:235], v4, s[20:23], s14 offen offset:1024 sc1\n"
"ds_read_b64_tr_b16 v[162:163], v20 offset:25600\n"
"s_waitcnt lgkmcnt(4)\n"
"s_setprio 0\n"
"v_exp_f32 v96, v96\n"
"v_exp_f32 v97, v97\n"
"v_exp_f32 v98, v98\n"
"v_exp_f32 v99, v99\n"
"v_mfma_f32_32x32x16_bf16 v[108:123], v[44:47], v[36:39], v[108:123]\n"
"s_cmp_lt_u32 s49, 28672\n"
"s_cselect_b32 s15, 0, 28672\n"
"s_sub_u32 s14, s14, s15\n"
"ds_read_b128 v[44:47], v12 offset:42064\n"
"buffer_load_dwordx4 v[236:239], v4, s[20:23], s14 offen sc1\n"
"v_exp_f32 v100, v100\n"
"v_exp_f32 v101, v101\n"
"v_exp_f32 v102, v102\n"
"v_exp_f32 v103, v103\n"
"v_smfmac_f32_32x32x32_bf16 v[76:91], v[48:51], v[92:99], v9\n"
"ds_read_b128 v[48:51], v14 offset:32832\n"
"s_waitcnt vmcnt(7) lgkmcnt(2)\n"
"s_barrier\n"
"s_setprio 3\n"
"v_exp_f32 v104, v104\n"
"v_exp_f32 v105, v105\n"
"v_exp_f32 v106, v106\n"
"v_exp_f32 v107, v107\n"
"v_mfma_f32_32x32x16_bf16 v[124:139], v[40:43], v[32:35], v[60:75]\n"
"s_add_u32 m0, s48, 16416\n"
"ds_read_b128 v[40:43], v12 offset:49248\n"
"buffer_load_dwordx4 v10, s[20:23], s88 offen sc1 lds\n"
"ds_read_b64_tr_b16 v[164:165], v21 offset:16384\n"
"v_exp_f32 v108, v108\n"
"v_exp_f32 v109, v109\n"
"v_exp_f32 v110, v110\n"
"v_exp_f32 v111, v111\n"
"v_smfmac_f32_32x32x32_bf16 v[76:91], v[52:55], v[100:107], v9\n"
"ds_read_b128 v[52:55], v16 offset:32832\n"
"buffer_load_dwordx4 v[240:243], v4, s[20:23], s14 offen offset:1024 sc1\n"
"ds_read_b64_tr_b16 v[166:167], v21 offset:17408\n"
"s_waitcnt lgkmcnt(4)\n"
"s_setprio 2\n"
"v_exp_f32 v112, v112\n"
"v_exp_f32 v113, v113\n"
"v_exp_f32 v114, v114\n"
"v_exp_f32 v115, v115\n"
"v_mfma_f32_32x32x16_bf16 v[124:139], v[44:47], v[36:39], v[124:139]\n"
"ds_read_b128 v[44:47], v12 offset:50272\n"
"s_sub_u32 s14, s49, s47\n"
"s_add_u32 s14, s14, 10485760\n"
"s_mov_b64 exec, s[8:9]\n"
"buffer_load_dwordx4 v[244:247], v57, s[20:23], s14 offen sc1\n"
"s_mov_b64 exec, -1\n"
"v_exp_f32 v116, v116\n"
"v_exp_f32 v117, v117\n"
"v_exp_f32 v118, v118\n"
"v_exp_f32 v119, v119\n"
"v_smfmac_f32_32x32x32_bf16 v[76:91], v[48:51], v[108:115], v9\n"
"ds_read_b128 v[48:51], v14 offset:41040\n"
"s_waitcnt lgkmcnt(3)\n"
"s_setprio 1\n"
"v_exp_f32 v120, v120\n"
"v_exp_f32 v121, v121\n"
"v_exp_f32 v122, v122\n"
"v_exp_f32 v123, v123\n"
"v_mfma_f32_32x32x16_bf16 v[92:107], v[40:43], v[32:35], v[60:75]\n"
"s_add_u32 m0, s48, 24624\n"
"ds_read_b128 v[40:43], v13 offset:0\n"
"buffer_load_dwordx4 v10, s[20:23], s89 offen sc1 lds\n"
"ds_read_b64_tr_b16 v[168:169], v21 offset:8192\n"
"v_exp_f32 v124, v124\n"
"v_exp_f32 v125, v125\n"
"v_exp_f32 v126, v126\n"
"v_exp_f32 v127, v127\n"
"v_smfmac_f32_32x32x32_bf16 v[76:91], v[52:55], v[116:123], v9\n"
"ds_read_b128 v[52:55], v16 offset:41040\n"
"s_add_u32 s14, s14, 14336\n"
"s_mov_b64 exec, s[8:9]\n"
"buffer_load_dwordx4 v[248:251], v57, s[20:23], s14 offen sc1\n"
"s_mov_b64 exec, -1\n"
"ds_read_b64_tr_b16 v[170:171], v21 offset:9216\n"
"s_waitcnt lgkmcnt(4)\n"
"s_setprio 0\n"
"v_exp_f32 v128, v128\n"
"v_exp_f32 v129, v129\n"
"v_exp_f32 v130, v130\n"
"v_exp_f32 v131, v131\n"
"v_mfma_f32_32x32x16_bf16 v[92:107], v[44:47], v[36:39], v[92:107]\n"
"ds_read_b128 v[44:47], v13 offset:1024\n"
"s_sub_u32 s14, s14, s15\n"
"s_mov_b64 exec, s[8:9]\n"
"buffer_load_dwordx4 v[252:255], v57, s[20:23], s14 offen sc1\n"
"s_mov_b64 exec, -1\n"
"v_exp_f32 v132, v132\n"
"v_exp_f32 v133, v133\n"
"v_exp_f32 v134, v134\n"
"v_exp_f32 v135, v135\n"
"v_smfmac_f32_32x32x32_bf16 v[76:91], v[48:51], v[124:131], v9\n"
"ds_read_b128 v[48:51], v14 offset:49248\n"
"s_waitcnt lgkmcnt(2)\n"
"s_barrier\n"
"s_setprio 3\n"
"v_exp_f32 v136, v136\n"
"v_exp_f32 v137, v137\n"
"v_exp_f32 v138, v138\n"
"v_exp_f32 v139, v139\n"
"v_mfma_f32_32x32x16_bf16 v[108:123], v[40:43], v[32:35], v[60:75]\n"
"ds_read_b128 v[40:43], v13 offset:8208\n"
"v_exp_f32 v92, v92\n"
"v_exp_f32 v93, v93\n"
"v_exp_f32 v94, v94\n"
"v_exp_f32 v95, v95\n"
"v_smfmac_f32_32x32x32_bf16 v[76:91], v[52:55], v[132:139], v9\n"
"s_add_u32 m0, s48, 32832\n"
"ds_read_b128 v[52:55], v16 offset:49248\n"
"buffer_load_dwordx4 v10, s[20:23], s90 offen sc1 lds\n"
"s_waitcnt lgkmcnt(2)\n"
"s_setprio 2\n"
"v_exp_f32 v96, v96\n"
"v_exp_f32 v97, v97\n"
"v_exp_f32 v98, v98\n"
"v_exp_f32 v99, v99\n"
"v_mfma_f32_32x32x16_bf16 v[108:123], v[44:47], v[36:39], v[108:123]\n"
"s_add_u32 m0, s48, 147616\n"
"s_mov_b32 s17, 229376\n"
"ds_read_b128 v[44:47], v13 offset:9232\n"
"buffer_load_dwordx4 v18, s[24:27], s17 offen lds\n"
"v_exp_f32 v100, v100\n"
"v_exp_f32 v101, v101\n"
"v_exp_f32 v102, v102\n"
"v_exp_f32 v103, v103\n"
"v_smfmac_f32_32x32x32_bf16 v[76:91], v[48:51], v[92:99], v9\n"
"ds_read_b128 v[48:51], v15 offset:0\n"
"s_waitcnt lgkmcnt(2)\n"
"s_setprio 1\n"
"v_exp_f32 v104, v104\n"
"v_exp_f32 v105, v105\n"
"v_exp_f32 v106, v106\n"
"v_exp_f32 v107, v107\n"
"s_waitcnt vmcnt(2)\n"
"s_setprio 3\n"
"v_cndmask_b32 v220, 0, v220, s[4:5]\n"
"v_cndmask_b32 v221, 0, v221, s[4:5]\n"
"v_cndmask_b32 v226, 0, v226, s[6:7]\n"
"v_cndmask_b32 v227, 0, v227, s[6:7]\n"
"s_mov_b32 s14, 0x3e8b97f6\n"
"v_mfma_f32_32x32x16_bf16 v[124:139], v[236:239], v[220:223], v[204:219]\n"
"v_mfma_f32_32x32x16_bf16 v[124:139], v[240:243], v[224:227], v[124:139]\n"
"v_mfma_f32_32x32x16_bf16 v[196:211], v[228:231], v[220:223], v[196:211]\n"
"v_mfma_f32_32x32x16_bf16 v[196:211], v[232:235], v[224:227], v[196:211]\n"
"v_mov_b32 v228, 0\n"
"v_mov_b32 v229, 0\n"
"v_mov_b32 v230, 0\n"
"v_mov_b32 v231, 0\n"
"v_mov_b32 v232, 0\n"
"v_mov_b32 v233, 0\n"
"v_mov_b32 v234, 0\n"
"v_mov_b32 v235, 0\n"
"v_mov_b32 v236, 0\n"
"v_mov_b32 v237, 0\n"
"v_mov_b32 v238, 0\n"
"v_mov_b32 v239, 0\n"
"v_mov_b32 v240, 0\n"
"v_mov_b32 v241, 0\n"
"v_mov_b32 v242, 0\n"
"v_mov_b32 v243, 0\n"
"v_max3_f32 v7, v132, v133, v134\n"
"v_max3_f32 v8, v135, v136, v137\n"
"v_max3_f32 v7, v7, v138, v139\n"
"v_max3_f32 v8, v8, v196, v197\n"
"v_max3_f32 v7, v7, v198, v199\n"
"v_max3_f32 v8, v8, v200, v201\n"
"v_max3_f32 v7, v7, v202, v203\n"
"v_max3_f32 v8, v8, v204, v205\n"
"v_max3_f32 v7, v7, v206, v207\n"
"v_max3_f32 v8, v8, v208, v209\n"
"v_max3_f32 v7, v7, v210, v211\n"
"v_max_f32 v7, v7, v8\n"
"v_mov_b32 v8, v7\n"
"s_nop 1\n"
"v_permlane32_swap_b32 v7, v8\n"
"v_max_f32 v7, v7, v8\n"
"v_mul_f32 v7, s14, v7\n"
"v_fma_f32 v132, v132, s14, -v7\n"
"v_fma_f32 v133, v133, s14, -v7\n"
"v_fma_f32 v134, v134, s14, -v7\n"
"v_fma_f32 v135, v135, s14, -v7\n"
"v_fma_f32 v136, v136, s14, -v7\n"
"v_fma_f32 v137, v137, s14, -v7\n"
"v_fma_f32 v138, v138, s14, -v7\n"
"v_fma_f32 v139, v139, s14, -v7\n"
"v_fma_f32 v196, v196, s14, -v7\n"
"v_fma_f32 v197, v197, s14, -v7\n"
"v_fma_f32 v198, v198, s14, -v7\n"
"v_fma_f32 v199, v199, s14, -v7\n"
"v_fma_f32 v200, v200, s14, -v7\n"
"v_fma_f32 v201, v201, s14, -v7\n"
"v_fma_f32 v202, v202, s14, -v7\n"
"v_fma_f32 v203, v203, s14, -v7\n"
"v_fma_f32 v204, v204, s14, -v7\n"
"v_fma_f32 v205, v205, s14, -v7\n"
"v_fma_f32 v206, v206, s14, -v7\n"
"v_fma_f32 v207, v207, s14, -v7\n"
"v_fma_f32 v208, v208, s14, -v7\n"
"v_fma_f32 v209, v209, s14, -v7\n"
"v_fma_f32 v210, v210, s14, -v7\n"
"v_fma_f32 v211, v211, s14, -v7\n"
"v_exp_f32 v132, v132\n"
"v_exp_f32 v133, v133\n"
"v_exp_f32 v134, v134\n"
"v_exp_f32 v135, v135\n"
"v_exp_f32 v136, v136\n"
"v_exp_f32 v137, v137\n"
"v_exp_f32 v138, v138\n"
"v_exp_f32 v139, v139\n"
"v_exp_f32 v196, v196\n"
"v_exp_f32 v197, v197\n"
"v_smfmac_f32_32x32x32_bf16 v[228:243], v[252:255], v[132:139], v9\n"
"v_exp_f32 v198, v198\n"
"v_exp_f32 v199, v199\n"
"v_exp_f32 v200, v200\n"
"v_exp_f32 v201, v201\n"
"v_exp_f32 v202, v202\n"
"v_exp_f32 v203, v203\n"
"v_exp_f32 v204, v204\n"
"v_exp_f32 v205, v205\n"
"v_smfmac_f32_32x32x32_bf16 v[228:243], v[244:247], v[196:203], v9\n"
"v_exp_f32 v206, v206\n"
"v_exp_f32 v207, v207\n"
"v_exp_f32 v208, v208\n"
"v_exp_f32 v209, v209\n"
"v_exp_f32 v210, v210\n"
"v_exp_f32 v211, v211\n"
"s_add_u32 s14, s49, 12582912\n"
"s_nop 0\n"
"v_smfmac_f32_32x32x32_bf16 v[228:243], v[248:251], v[204:211], v9\n"
"s_nop 9\n"
"s_cmp_eq_u32 s43, 0\n"
"s_cselect_b64 vcc, -1, 0\n"
"v_cndmask_b32 v7, v228, v240, vcc\n"
"v_mov_b32 v8, v7\n"
"s_nop 1\n"
"v_permlane32_swap_b32 v7, v8\n"
"v_cndmask_b32 v7, v7, v8, vcc\n"
"v_rcp_f32 v7, v7\n"
"s_nop 0\n"
"v_mul_f32 v228, v228, v7\n"
"v_mul_f32 v229, v229, v7\n"
"v_mul_f32 v230, v230, v7\n"
"v_mul_f32 v231, v231, v7\n"
"v_mul_f32 v232, v232, v7\n"
"v_mul_f32 v233, v233, v7\n"
"v_mul_f32 v234, v234, v7\n"
"v_mul_f32 v235, v235, v7\n"
"v_mul_f32 v236, v236, v7\n"
"v_mul_f32 v237, v237, v7\n"
"v_mul_f32 v238, v238, v7\n"
"v_mul_f32 v239, v239, v7\n"
"v_mul_f32 v240, v240, v7\n"
"v_mul_f32 v241, v241, v7\n"
"v_mul_f32 v242, v242, v7\n"
"v_mul_f32 v243, v243, v7\n"
"v_cvt_pk_bf16_f32 v220, v228, v229\n"
"v_cvt_pk_bf16_f32 v221, v230, v231\n"
"v_cvt_pk_bf16_f32 v222, v232, v233\n"
"v_cvt_pk_bf16_f32 v223, v234, v235\n"
"v_cvt_pk_bf16_f32 v224, v236, v237\n"
"v_cvt_pk_bf16_f32 v225, v238, v239\n"
"v_cvt_pk_bf16_f32 v226, v240, v241\n"
"v_cvt_pk_bf16_f32 v227, v242, v243\n"
"v_min_u32 v220, -2, v220\n"
"v_min_u32 v221, -2, v221\n"
"v_min_u32 v222, -2, v222\n"
"v_min_u32 v223, -2, v223\n"
"v_min_u32 v224, -2, v224\n"
"v_min_u32 v225, -2, v225\n"
"v_min_u32 v226, -2, v226\n"
"v_min_u32 v227, -2, v227\n"
"v_permlane32_swap_b32 v220, v222\n"
"v_permlane32_swap_b32 v221, v223\n"
"v_permlane32_swap_b32 v224, v226\n"
"v_permlane32_swap_b32 v225, v227\n"
"v_cndmask_b32 v24, v222, v224, vcc\n"
"v_cndmask_b32 v25, v223, v225, vcc\n"
"s_setprio 1\n"
"v_mfma_f32_32x32x16_bf16 v[124:139], v[40:43], v[32:35], v[60:75]\n"
"s_add_u32 m0, s48, 41040\n"
"ds_read_b128 v[40:43], v13 offset:16416\n"
"buffer_load_dwordx4 v10, s[20:23], s91 offen sc1 lds\n"
"v_exp_f32 v108, v108\n"
"v_exp_f32 v109, v109\n"
"v_exp_f32 v110, v110\n"
"v_exp_f32 v111, v111\n"
"v_smfmac_f32_32x32x32_bf16 v[76:91], v[52:55], v[100:107], v9\n"
"ds_read_b128 v[52:55], v17 offset:0\n"
"buffer_store_dwordx4 v[220:223], v56, s[20:23], s14 offen sc1\n"
"s_waitcnt lgkmcnt(2)\n"
"s_setprio 0\n"
"v_exp_f32 v112, v112\n"
"v_exp_f32 v113, v113\n"
"v_exp_f32 v114, v114\n"
"v_exp_f32 v115, v115\n"
"v_mfma_f32_32x32x16_bf16 v[124:139], v[44:47], v[36:39], v[124:139]\n"
"s_add_u32 m0, s48, 139424\n"
"s_mov_b32 s17, 258048\n"
"ds_read_b128 v[44:47], v13 offset:17440\n"
"buffer_load_dwordx4 v18, s[24:27], s17 offen lds\n"
"v_exp_f32 v116, v116\n"
"v_exp_f32 v117, v117\n"
"v_exp_f32 v118, v118\n"
"v_exp_f32 v119, v119\n"
"v_smfmac_f32_32x32x32_bf16 v[76:91], v[48:51], v[108:115], v9\n"
"ds_read_b128 v[48:51], v15 offset:8208\n"
"s_waitcnt lgkmcnt(2)\n"
"s_barrier\n"
"s_setprio 3\n"
"v_exp_f32 v120, v120\n"
"v_exp_f32 v121, v121\n"
"v_exp_f32 v122, v122\n"
"v_exp_f32 v123, v123\n"
"v_mfma_f32_32x32x16_bf16 v[92:107], v[40:43], v[32:35], v[60:75]\n"
"s_add_u32 m0, s48, 49248\n"
"ds_read_b128 v[40:43], v12 offset:0\n"
"buffer_load_dwordx4 v10, s[20:23], s92 offen sc1 lds\n"
"v_exp_f32 v124, v124\n"
"v_exp_f32 v125, v125\n"
"v_exp_f32 v126, v126\n"
"v_exp_f32 v127, v127\n"
"v_smfmac_f32_32x32x32_bf16 v[76:91], v[52:55], v[116:123], v9\n"
"ds_read_b128 v[52:55], v17 offset:8208\n"
"buffer_store_dwordx4 v[224:227], v22, s[20:23], s14 offen offset:1024 sc1\n"
"s_waitcnt lgkmcnt(2)\n"
"s_setprio 2\n"
"v_exp_f32 v128, v128\n"
"v_exp_f32 v129, v129\n"
"v_exp_f32 v130, v130\n"
"v_exp_f32 v131, v131\n"
"v_mfma_f32_32x32x16_bf16 v[92:107], v[44:47], v[36:39], v[92:107]\n"
"ds_read_b128 v[44:47], v12 offset:1024\n"
"buffer_store_dwordx2 v[24:25], v11, s[20:23], s14 offen sc1\n"
"v_exp_f32 v132, v132\n"
"v_exp_f32 v133, v133\n"
"v_exp_f32 v134, v134\n"
"v_exp_f32 v135, v135\n"
"v_smfmac_f32_32x32x32_bf16 v[76:91], v[48:51], v[124:131], v9\n"
"s_add_u32 m0, s48, 98464\n"
"s_mov_b32 s17, 286720\n"
"ds_read_b128 v[48:51], v15 offset:16416\n"
"buffer_load_dwordx4 v18, s[24:27], s17 offen lds\n"
"s_waitcnt lgkmcnt(2)\n"
"s_setprio 1\n"
"v_exp_f32 v136, v136\n"
"v_exp_f32 v137, v137\n"
"v_exp_f32 v138, v138\n"
"v_exp_f32 v139, v139\n"
"v_mfma_f32_32x32x16_bf16 v[108:123], v[40:43], v[32:35], v[60:75]\n"
"ds_read_b128 v[40:43], v12 offset:8208\n"
"v_exp_f32 v92, v92\n"
"v_exp_f32 v93, v93\n"
"v_exp_f32 v94, v94\n"
"v_exp_f32 v95, v95\n"
"v_smfmac_f32_32x32x32_bf16 v[76:91], v[52:55], v[132:139], v9\n"
"s_add_u32 m0, s48, 57456\n"
"ds_read_b128 v[52:55], v17 offset:16416\n"
"buffer_load_dwordx4 v10, s[20:23], s93 offen sc1 lds\n"
"s_waitcnt lgkmcnt(2)\n"
"s_setprio 0\n"
"v_exp_f32 v96, v96\n"
"v_exp_f32 v97, v97\n"
"v_exp_f32 v98, v98\n"
"v_exp_f32 v99, v99\n"
"v_mfma_f32_32x32x16_bf16 v[108:123], v[44:47], v[36:39], v[108:123]\n"
"s_add_u32 m0, s48, 106656\n"
"s_mov_b32 s17, 315392\n"
"ds_read_b128 v[44:47], v12 offset:9232\n"
"buffer_load_dwordx4 v18, s[24:27], s17 offen lds\n"
"v_exp_f32 v100, v100\n"
"v_exp_f32 v101, v101\n"
"v_exp_f32 v102, v102\n"
"v_exp_f32 v103, v103\n"
"v_smfmac_f32_32x32x32_bf16 v[76:91], v[48:51], v[92:99], v9\n"
"ds_read_b128 v[48:51], v14 offset:0\n"
"s_waitcnt vmcnt(6) lgkmcnt(2)\n"
"s_barrier\n"
"s_setprio 3\n"
"v_exp_f32 v104, v104\n"
"v_exp_f32 v105, v105\n"
"v_exp_f32 v106, v106\n"
"v_exp_f32 v107, v107\n"
"v_mfma_f32_32x32x16_bf16 v[124:139], v[40:43], v[32:35], v[60:75]\n"
"ds_read_b128 v[40:43], v12 offset:16416\n"
"ds_read_b64_tr_b16 v[172:173], v21 offset:16384\n"
"v_exp_f32 v108, v108\n"
"v_exp_f32 v109, v109\n"
"v_exp_f32 v110, v110\n"
"v_exp_f32 v111, v111\n"
"v_smfmac_f32_32x32x32_bf16 v[76:91], v[52:55], v[100:107], v9\n"
"s_add_u32 m0, s48, 65664\n"
"ds_read_b128 v[52:55], v16 offset:0\n"
"buffer_load_dwordx4 v10, s[20:23], s94 offen sc1 lds\n"
"ds_read_b64_tr_b16 v[174:175], v21 offset:17408\n"
"s_waitcnt lgkmcnt(4)\n"
"s_setprio 2\n"
"v_exp_f32 v112, v112\n"
"v_exp_f32 v113, v113\n"
"v_exp_f32 v114, v114\n"
"v_exp_f32 v115, v115\n"
"v_mfma_f32_32x32x16_bf16 v[124:139], v[44:47], v[36:39], v[124:139]\n"
"s_add_u32 m0, s48, 82080\n"
"s_mov_b32 s17, 344064\n"
"ds_read_b128 v[44:47], v12 offset:17440\n"
"buffer_load_dwordx4 v18, s[24:27], s17 offen lds\n"
"v_exp_f32 v116, v116\n"
"v_exp_f32 v117, v117\n"
"v_exp_f32 v118, v118\n"
"v_exp_f32 v119, v119\n"
"v_smfmac_f32_32x32x32_bf16 v[76:91], v[48:51], v[108:115], v9\n"
"ds_read_b128 v[48:51], v14 offset:8208\n"
"s_waitcnt lgkmcnt(3)\n"
"s_setprio 1\n"
"v_exp_f32 v120, v120\n"
"v_exp_f32 v121, v121\n"
"v_exp_f32 v122, v122\n"
"v_exp_f32 v123, v123\n"
"v_mfma_f32_32x32x16_bf16 v[92:107], v[40:43], v[32:35], v[60:75]\n"
"ds_read_b128 v[40:43], v12 offset:24624\n"
"ds_read_b64_tr_b16 v[176:177], v21 offset:8192\n"
"v_exp_f32 v124, v124\n"
"v_exp_f32 v125, v125\n"
"v_exp_f32 v126, v126\n"
"v_exp_f32 v127, v127\n"
"v_smfmac_f32_32x32x32_bf16 v[76:91], v[52:55], v[116:123], v9\n"
"s_add_u32 m0, s48, 73872\n"
"ds_read_b128 v[52:55], v16 offset:8208\n"
"buffer_load_dwordx4 v10, s[20:23], s95 offen sc1 lds\n"
"ds_read_b64_tr_b16 v[178:179], v21 offset:9216\n"
"s_waitcnt lgkmcnt(4)\n"
"s_setprio 0\n"
"v_exp_f32 v128, v128\n"
"v_exp_f32 v129, v129\n"
"v_exp_f32 v130, v130\n"
"v_exp_f32 v131, v131\n"
"v_mfma_f32_32x32x16_bf16 v[92:107], v[44:47], v[36:39], v[92:107]\n"
"s_add_u32 m0, s48, 90272\n"
"s_mov_b32 s17, 372736\n"
"ds_read_b128 v[44:47], v12 offset:25648\n"
"buffer_load_dwordx4 v18, s[24:27], s17 offen lds\n"
"v_exp_f32 v132, v132\n"
"v_exp_f32 v133, v133\n"
"v_exp_f32 v134, v134\n"
"v_exp_f32 v135, v135\n"
"v_smfmac_f32_32x32x32_bf16 v[76:91], v[48:51], v[124:131], v9\n"
"ds_read_b128 v[48:51], v14 offset:16416\n"
"s_waitcnt vmcnt(4) lgkmcnt(2)\n"
"s_barrier\n"
"s_setprio 3\n"
"v_exp_f32 v136, v136\n"
"v_exp_f32 v137, v137\n"
"v_exp_f32 v138, v138\n"
"v_exp_f32 v139, v139\n"
"v_mfma_f32_32x32x16_bf16 v[108:123], v[40:43], v[32:35], v[60:75]\n"
"ds_read_b128 v[40:43], v12 offset:32832\n"
"ds_read_b64_tr_b16 v[180:181], v20 offset:16384\n"
"v_exp_f32 v92, v92\n"
"v_exp_f32 v93, v93\n"
"v_exp_f32 v94, v94\n"
"v_exp_f32 v95, v95\n"
"v_smfmac_f32_32x32x32_bf16 v[76:91], v[52:55], v[132:139], v9\n"
"s_add_u32 m0, s48, 147616\n"
"s_mov_b32 s17, 0\n"
"ds_read_b128 v[52:55], v16 offset:16416\n"
"buffer_load_dwordx4 v19, s[28:31], s17 offen lds\n"
"ds_read_b64_tr_b16 v[182:183], v20 offset:17408\n"
"s_waitcnt lgkmcnt(4)\n"
"s_setprio 2\n"
"v_exp_f32 v96, v96\n"
"v_exp_f32 v97, v97\n"
"v_exp_f32 v98, v98\n"
"v_exp_f32 v99, v99\n"
"v_mfma_f32_32x32x16_bf16 v[108:123], v[44:47], v[36:39], v[108:123]\n"
"s_add_u32 m0, s48, 139424\n"
"s_mov_b32 s17, 57344\n"
"ds_read_b128 v[44:47], v12 offset:33856\n"
"buffer_load_dwordx4 v19, s[28:31], s17 offen lds\n"
"v_exp_f32 v100, v100\n"
"v_exp_f32 v101, v101\n"
"v_exp_f32 v102, v102\n"
"v_exp_f32 v103, v103\n"
"v_smfmac_f32_32x32x32_bf16 v[76:91], v[48:51], v[92:99], v9\n"
"ds_read_b128 v[48:51], v14 offset:24624\n"
"s_waitcnt lgkmcnt(3)\n"
"s_setprio 1\n"
"v_exp_f32 v104, v104\n"
"v_exp_f32 v105, v105\n"
"v_exp_f32 v106, v106\n"
"v_exp_f32 v107, v107\n"
"v_mfma_f32_32x32x16_bf16 v[124:139], v[40:43], v[32:35], v[60:75]\n"
"ds_read_b128 v[40:43], v12 offset:41040\n"
"ds_read_b64_tr_b16 v[184:185], v20 offset:24576\n"
"v_exp_f32 v108, v108\n"
"v_exp_f32 v109, v109\n"
"v_exp_f32 v110, v110\n"
"v_exp_f32 v111, v111\n"
"v_smfmac_f32_32x32x32_bf16 v[76:91], v[52:55], v[100:107], v9\n"
"s_add_u32 m0, s48, 131232\n"
"s_mov_b32 s17, 114688\n"
"ds_read_b128 v[52:55], v16 offset:24624\n"
"buffer_load_dwordx4 v19, s[28:31], s17 offen lds\n"
"ds_read_b64_tr_b16 v[186:187], v20 offset:25600\n"
"s_waitcnt lgkmcnt(4)\n"
"s_setprio 0\n"
"v_exp_f32 v112, v112\n"
"v_exp_f32 v113, v113\n"
"v_exp_f32 v114, v114\n"
"v_exp_f32 v115, v115\n"
"v_mfma_f32_32x32x16_bf16 v[124:139], v[44:47], v[36:39], v[124:139]\n"
"s_add_u32 m0, s48, 123040\n"
"s_mov_b32 s17, 172032\n"
"ds_read_b128 v[44:47], v12 offset:42064\n"
"buffer_load_dwordx4 v19, s[28:31], s17 offen lds\n"
"v_exp_f32 v116, v116\n"
"v_exp_f32 v117, v117\n"
"v_exp_f32 v118, v118\n"
"v_exp_f32 v119, v119\n"
"v_smfmac_f32_32x32x32_bf16 v[76:91], v[48:51], v[108:115], v9\n"
"ds_read_b128 v[48:51], v14 offset:32832\n"
"s_waitcnt vmcnt(4) lgkmcnt(2)\n"
"s_barrier\n"
"s_setprio 3\n"
"v_exp_f32 v120, v120\n"
"v_exp_f32 v121, v121\n"
"v_exp_f32 v122, v122\n"
"v_exp_f32 v123, v123\n"
"v_mfma_f32_32x32x16_bf16 v[92:107], v[40:43], v[32:35], v[60:75]\n"
"ds_read_b128 v[40:43], v12 offset:49248\n"
"ds_read_b64_tr_b16 v[188:189], v20 offset:0\n"
"v_exp_f32 v124, v124\n"
"v_exp_f32 v125, v125\n"
"v_exp_f32 v126, v126\n"
"v_exp_f32 v127, v127\n"
"v_smfmac_f32_32x32x32_bf16 v[76:91], v[52:55], v[116:123], v9\n"
"s_add_u32 m0, s48, 114848\n"
"s_mov_b32 s17, 229376\n"
"ds_read_b128 v[52:55], v16 offset:32832\n"
"buffer_load_dwordx4 v19, s[28:31], s17 offen lds\n"
"ds_read_b64_tr_b16 v[190:191], v20 offset:1024\n"
"s_waitcnt lgkmcnt(4)\n"
"s_setprio 2\n"
"v_exp_f32 v128, v128\n"
"v_exp_f32 v129, v129\n"
"v_exp_f32 v130, v130\n"
"v_exp_f32 v131, v131\n"
"v_mfma_f32_32x32x16_bf16 v[92:107], v[44:47], v[36:39], v[92:107]\n"
"s_add_u32 m0, s48, 106656\n"
"s_mov_b32 s17, 286720\n"
"ds_read_b128 v[44:47], v12 offset:50272\n"
"buffer_load_dwordx4 v19, s[28:31], s17 offen lds\n"
"v_exp_f32 v132, v132\n"
"v_exp_f32 v133, v133\n"
"v_exp_f32 v134, v134\n"
"v_exp_f32 v135, v135\n"
"v_smfmac_f32_32x32x32_bf16 v[76:91], v[48:51], v[124:131], v9\n"
"ds_read_b128 v[48:51], v14 offset:41040\n"
"s_waitcnt lgkmcnt(3)\n"
"s_setprio 1\n"
"v_exp_f32 v136, v136\n"
"v_exp_f32 v137, v137\n"
"v_exp_f32 v138, v138\n"
"v_exp_f32 v139, v139\n"
"v_mfma_f32_32x32x16_bf16 v[108:123], v[40:43], v[32:35], v[60:75]\n"
"ds_read_b128 v[40:43], v13 offset:0\n"
"ds_read_b64_tr_b16 v[192:193], v20 offset:8192\n"
"v_exp_f32 v92, v92\n"
"v_exp_f32 v93, v93\n"
"v_exp_f32 v94, v94\n"
"v_exp_f32 v95, v95\n"
"v_smfmac_f32_32x32x32_bf16 v[76:91], v[52:55], v[132:139], v9\n"
"s_add_u32 m0, s48, 98464\n"
"s_mov_b32 s17, 344064\n"
"ds_read_b128 v[52:55], v16 offset:41040\n"
"buffer_load_dwordx4 v19, s[28:31], s17 offen lds\n"
"ds_read_b64_tr_b16 v[194:195], v20 offset:9216\n"
"s_waitcnt lgkmcnt(4)\n"
"s_setprio 0\n"
"v_exp_f32 v96, v96\n"
"v_exp_f32 v97, v97\n"
"v_exp_f32 v98, v98\n"
"v_exp_f32 v99, v99\n"
"v_mfma_f32_32x32x16_bf16 v[108:123], v[44:47], v[36:39], v[108:123]\n"
"s_add_u32 s16, s10, s48\n"
"s_add_u32 m0, s48, 0\n"
"s_add_u32 s17, s16, 0\n"
"ds_read_b128 v[44:47], v13 offset:1024\n"
"buffer_load_dwordx4 v4, s[20:23], s17 offen sc1 lds\n"
"v_exp_f32 v100, v100\n"
"v_exp_f32 v101, v101\n"
"v_exp_f32 v102, v102\n"
"v_exp_f32 v103, v103\n"
"v_smfmac_f32_32x32x32_bf16 v[76:91], v[48:51], v[92:99], v9\n"
"ds_read_b128 v[48:51], v14 offset:49248\n"
"s_waitcnt lgkmcnt(2)\n"
"s_barrier\n"
"s_setprio 3\n"
"v_exp_f32 v104, v104\n"
"v_exp_f32 v105, v105\n"
"v_exp_f32 v106, v106\n"
"v_exp_f32 v107, v107\n"
"v_mfma_f32_32x32x16_bf16 v[124:139], v[40:43], v[32:35], v[60:75]\n"
"ds_read_b128 v[40:43], v13 offset:8208\n"
"v_exp_f32 v108, v108\n"
"v_exp_f32 v109, v109\n"
"v_exp_f32 v110, v110\n"
"v_exp_f32 v111, v111\n"
"v_smfmac_f32_32x32x32_bf16 v[76:91], v[52:55], v[100:107], v9\n"
"s_add_u32 m0, s48, 8192\n"
"s_add_u32 s17, s16, 8192\n"
"ds_read_b128 v[52:55], v16 offset:49248\n"
"buffer_load_dwordx4 v4, s[20:23], s17 offen sc1 lds\n"
"s_waitcnt lgkmcnt(2)\n"
"s_setprio 2\n"
"v_exp_f32 v112, v112\n"
"v_exp_f32 v113, v113\n"
"v_exp_f32 v114, v114\n"
"v_exp_f32 v115, v115\n"
"v_mfma_f32_32x32x16_bf16 v[124:139], v[44:47], v[36:39], v[124:139]\n"
"s_add_u32 m0, s48, 16384\n"
"s_add_u32 s17, s16, 16384\n"
"ds_read_b128 v[44:47], v13 offset:9232\n"
"buffer_load_dwordx4 v4, s[20:23], s17 offen sc1 lds\n"
"v_exp_f32 v116, v116\n"
"v_exp_f32 v117, v117\n"
"v_exp_f32 v118, v118\n"
"v_exp_f32 v119, v119\n"
"v_smfmac_f32_32x32x32_bf16 v[76:91], v[48:51], v[108:115], v9\n"
"ds_read_b128 v[48:51], v15 offset:0\n"
"s_waitcnt lgkmcnt(2)\n"
"s_setprio 1\n"
"v_exp_f32 v120, v120\n"
"v_exp_f32 v121, v121\n"
"v_exp_f32 v122, v122\n"
"v_exp_f32 v123, v123\n"
"v_mfma_f32_32x32x16_bf16 v[92:107], v[40:43], v[32:35], v[60:75]\n"
"ds_read_b128 v[40:43], v13 offset:16416\n"
"v_exp_f32 v124, v124\n"
"v_exp_f32 v125, v125\n"
"v_exp_f32 v126, v126\n"
"v_exp_f32 v127, v127\n"
"v_smfmac_f32_32x32x32_bf16 v[76:91], v[52:55], v[116:123], v9\n"
"s_add_u32 m0, s48, 24576\n"
"s_add_u32 s17, s16, 24576\n"
"ds_read_b128 v[52:55], v17 offset:0\n"
"buffer_load_dwordx4 v4, s[20:23], s17 offen sc1 lds\n"
"s_waitcnt lgkmcnt(2)\n"
"s_setprio 0\n"
"v_exp_f32 v128, v128\n"
"v_exp_f32 v129, v129\n"
"v_exp_f32 v130, v130\n"
"v_exp_f32 v131, v131\n"
"v_mfma_f32_32x32x16_bf16 v[92:107], v[44:47], v[36:39], v[92:107]\n"
"s_add_u32 m0, s48, 32768\n"
"s_add_u32 s17, s16, 32768\n"
"ds_read_b128 v[44:47], v13 offset:17440\n"
"buffer_load_dwordx4 v4, s[20:23], s17 offen sc1 lds\n"
"v_exp_f32 v132, v132\n"
"v_exp_f32 v133, v133\n"
"v_exp_f32 v134, v134\n"
"v_exp_f32 v135, v135\n"
"v_smfmac_f32_32x32x32_bf16 v[76:91], v[48:51], v[124:131], v9\n"
"ds_read_b128 v[48:51], v15 offset:8208\n"
"s_waitcnt lgkmcnt(2)\n"
"s_barrier\n"
"s_setprio 3\n"
"v_exp_f32 v136, v136\n"
"v_exp_f32 v137, v137\n"
"v_exp_f32 v138, v138\n"
"v_exp_f32 v139, v139\n"
"v_mfma_f32_32x32x16_bf16 v[108:123], v[40:43], v[32:35], v[60:75]\n"
"v_exp_f32 v92, v92\n"
"v_exp_f32 v93, v93\n"
"v_exp_f32 v94, v94\n"
"v_exp_f32 v95, v95\n"
"v_smfmac_f32_32x32x32_bf16 v[76:91], v[52:55], v[132:139], v9\n"
"s_add_u32 m0, s48, 40960\n"
"s_add_u32 s17, s16, 40960\n"
"ds_read_b128 v[52:55], v17 offset:8208\n"
"buffer_load_dwordx4 v4, s[20:23], s17 offen sc1 lds\n"
"s_waitcnt lgkmcnt(1)\n"
"s_setprio 2\n"
"v_exp_f32 v96, v96\n"
"v_exp_f32 v97, v97\n"
"v_exp_f32 v98, v98\n"
"v_exp_f32 v99, v99\n"
"v_mfma_f32_32x32x16_bf16 v[108:123], v[44:47], v[36:39], v[108:123]\n"
"s_add_u32 m0, s48, 49152\n"
"s_add_u32 s17, s16, 49152\n"
"buffer_load_dwordx4 v4, s[20:23], s17 offen sc1 lds\n"
"v_exp_f32 v100, v100\n"
"v_exp_f32 v101, v101\n"
"v_exp_f32 v102, v102\n"
"v_exp_f32 v103, v103\n"
"v_smfmac_f32_32x32x32_bf16 v[76:91], v[48:51], v[92:99], v9\n"
"ds_read_b128 v[48:51], v15 offset:16416\n"
"s_waitcnt lgkmcnt(1)\n"
"s_setprio 1\n"
"v_exp_f32 v104, v104\n"
"v_exp_f32 v105, v105\n"
"v_exp_f32 v106, v106\n"
"v_exp_f32 v107, v107\n"
"s_nop 0\n"
"v_exp_f32 v108, v108\n"
"v_exp_f32 v109, v109\n"
"v_exp_f32 v110, v110\n"
"v_exp_f32 v111, v111\n"
"v_smfmac_f32_32x32x32_bf16 v[76:91], v[52:55], v[100:107], v9\n"
"ds_read_b128 v[52:55], v17 offset:16416\n"
"s_waitcnt lgkmcnt(1)\n"
"s_setprio 0\n"
"v_exp_f32 v112, v112\n"
"v_exp_f32 v113, v113\n"
"v_exp_f32 v114, v114\n"
"v_exp_f32 v115, v115\n"
"v_exp_f32 v116, v116\n"
"v_exp_f32 v117, v117\n"
"v_exp_f32 v118, v118\n"
"v_exp_f32 v119, v119\n"
"v_smfmac_f32_32x32x32_bf16 v[76:91], v[48:51], v[108:115], v9\n"
"s_waitcnt lgkmcnt(0)\n"
"s_setprio 3\n"
"v_exp_f32 v120, v120\n"
"v_exp_f32 v121, v121\n"
"v_exp_f32 v122, v122\n"
"v_exp_f32 v123, v123\n"
"s_nop 1\n"
"v_smfmac_f32_32x32x32_bf16 v[76:91], v[52:55], v[116:123], v9\n"
"s_setprio 0\n"
"s_add_u32 s14, s49, 14680064\n"
"s_cmp_eq_u32 s43, 1\n"
"s_cselect_b32 s15, 2130706432, s14\n"
"s_cmp_eq_u32 s43, 0\n"
"s_cselect_b64 vcc, -1, 0\n"
"s_nop 5\n"
"ds_write_b128 v28, v[76:79]\n"
"v_cndmask_b32 v5, v76, v88, vcc\n"
"ds_write_b128 v28, v[80:83] offset:1024\n"
"ds_write_b32 v30, v5\n"
"s_waitcnt vmcnt(0) lgkmcnt(0)\n"
"s_barrier\n"
"ds_read2st64_b32 v[24:25], v31 offset0:0 offset1:1\n"
"ds_read_b128 v[92:95], v29\n"
"ds_read_b128 v[96:99], v29 offset:1024\n"
"s_waitcnt lgkmcnt(2)\n"
"v_add_f32 v7, v24, v25\n"
"v_cmp_nlt_f32 vcc, 0x7800000, v7\n"
"v_rcp_f32 v6, v7\n"
"s_waitcnt lgkmcnt(0)\n"
"v_add_f32 v92, v92, v84\n"
"v_add_f32 v93, v93, v85\n"
"v_add_f32 v94, v94, v86\n"
"v_add_f32 v95, v95, v87\n"
"v_add_f32 v96, v96, v88\n"
"v_add_f32 v97, v97, v89\n"
"v_add_f32 v98, v98, v90\n"
"v_add_f32 v99, v99, v91\n"
"v_add_f32 v8, abs(v92), v7\n"
"v_add_f32 v5, abs(v93), abs(v94)\n"
"v_add_f32 v8, abs(v95), v8\n"
"v_add_f32 v5, abs(v96), v5\n"
"v_add_f32 v8, abs(v97), v8\n"
"v_add_f32 v5, abs(v98), v5\n"
"v_add_f32 v8, abs(v99), v8\n"
"s_mov_b64 s[52:53], vcc\n"
"v_add_f32 v8, v5, v8\n"
"v_cmp_ngt_f32 vcc, 0x7b800000, v8\n"
"s_or_b64 vcc, vcc, s[52:53]\n"
"s_cbranch_vccnz .Lfused_fwd_slow\n"
".Lfused_fwd_slow_r1:\n"
"v_mul_f32 v92, v92, v6\n"
"v_mul_f32 v93, v93, v6\n"
"v_mul_f32 v94, v94, v6\n"
"v_mul_f32 v95, v95, v6\n"
"v_mul_f32 v96, v96, v6\n"
"v_mul_f32 v97, v97, v6\n"
"v_mul_f32 v98, v98, v6\n"
"v_mul_f32 v99, v99, v6\n"
"v_cvt_pk_bf16_f32 v100, v92, v93\n"
"v_cvt_pk_bf16_f32 v101, v94, v95\n"
"v_cvt_pk_bf16_f32 v102, v96, v97\n"
"v_cvt_pk_bf16_f32 v103, v98, v99\n"
"v_min_u32 v100, -2, v100\n"
"v_min_u32 v102, -2, v102\n"
"v_min_u32 v101, -2, v101\n"
"v_min_u32 v103, -2, v103\n"
"v_permlane32_swap_b32 v100, v102\n"
"s_nop 0\n"
"v_permlane32_swap_b32 v101, v103\n"
"buffer_store_dwordx4 v[100:103], v22, s[20:23], s14 offen offset:1024 sc1\n"
"buffer_store_dwordx2 v[100:101], v11, s[20:23], s15 offen sc1\n"
"s_cmp_lt_u32 s40, 28\n"
"s_cselect_b32 s12, 0, 2130706432\n"
"s_cbranch_scc0 .Lfused_fwd_final_l\n"
"s_and_b32 s50, s38, 1\n"
"s_mul_i32 s15, s50, 28672\n"
"v_add_u32 v5, s15, v4\n"
"v_bfe_u32 v6, v1, 2, 2\n"
"v_lshl_add_u32 v7, v2, 3, v6\n"
"v_lshlrev_b32 v7, 7, v7\n"
"v_xor_b32 v6, s38, v6\n"
"v_and_b32 v6, 2, v6\n"
"v_lshl_add_u32 v7, v6, 5, v7\n"
"v_bfe_u32 v6, v1, 4, 1\n"
"v_lshl_add_u32 v7, v6, 5, v7\n"
"v_and_b32 v6, 3, v1\n"
"v_lshl_add_u32 v7, v6, 3, v7\n"
"v_add_u32 v24, 98464, v7\n"
"s_mov_b32 s63, 0\n"
".Lfused_fwd_aproj:\n"
"v_mov_b32 v8, 0\n"
"ds_read_b128 v[196:199], v5 offset:0\n"
"ds_read_b64_tr_b16 v[200:201], v24 offset:49152\n"
"ds_read_b64_tr_b16 v[202:203], v24 offset:49664\n"
"ds_read_b128 v[204:207], v5 offset:1024\n"
"ds_read_b64_tr_b16 v[208:209], v24 offset:51200\n"
"ds_read_b64_tr_b16 v[210:211], v24 offset:51712\n"
"ds_read_b128 v[212:215], v5 offset:2048\n"
"ds_read_b64_tr_b16 v[216:217], v24 offset:53248\n"
"ds_read_b64_tr_b16 v[218:219], v24 offset:53760\n"
"s_waitcnt lgkmcnt(6)\n"
"v_mfma_f32_32x32x16_bf16 v[60:75], v[200:203], v[196:199], 0\n"
"ds_read_b128 v[220:223], v5 offset:3072\n"
"ds_read_b64_tr_b16 v[224:225], v24 offset:55296\n"
"ds_read_b64_tr_b16 v[226:227], v24 offset:55808\n"
"v_max3_u32 v8, v8, v196, v197\n"
"v_max3_u32 v8, v8, v198, v199\n"
"s_waitcnt lgkmcnt(6)\n"
"v_mfma_f32_32x32x16_bf16 v[60:75], v[208:211], v[204:207], v[60:75]\n"
"ds_read_b128 v[196:199], v5 offset:4096\n"
"ds_read_b64_tr_b16 v[200:201], v24 offset:40960\n"
"ds_read_b64_tr_b16 v[202:203], v24 offset:41472\n"
"v_max3_u32 v8, v8, v204, v205\n"
"v_max3_u32 v8, v8, v206, v207\n"
"s_waitcnt lgkmcnt(6)\n"
"v_mfma_f32_32x32x16_bf16 v[60:75], v[216:219], v[212:215], v[60:75]\n"
"ds_read_b128 v[204:207], v5 offset:5120\n"
"ds_read_b64_tr_b16 v[208:209], v24 offset:43008\n"
"ds_read_b64_tr_b16 v[210:211], v24 offset:43520\n"
"v_max3_u32 v8, v8, v212, v213\n"
"v_max3_u32 v8, v8, v214, v215\n"
"s_waitcnt lgkmcnt(6)\n"
"v_mfma_f32_32x32x16_bf16 v[60:75], v[224:227], v[220:223], v[60:75]\n"
"ds_read_b128 v[212:215], v5 offset:6144\n"
"ds_read_b64_tr_b16 v[216:217], v24 offset:45056\n"
"ds_read_b64_tr_b16 v[218:219], v24 offset:45568\n"
"v_max3_u32 v8, v8, v220, v221\n"
"v_max3_u32 v8, v8, v222, v223\n"
"s_waitcnt lgkmcnt(6)\n"
"v_mfma_f32_32x32x16_bf16 v[60:75], v[200:203], v[196:199], v[60:75]\n"
"ds_read_b128 v[220:223], v5 offset:7168\n"
"ds_read_b64_tr_b16 v[224:225], v24 offset:47104\n"
"ds_read_b64_tr_b16 v[226:227], v24 offset:47616\n"
"v_max3_u32 v8, v8, v196, v197\n"
"v_max3_u32 v8, v8, v198, v199\n"
"s_waitcnt lgkmcnt(6)\n"
"v_mfma_f32_32x32x16_bf16 v[60:75], v[208:211], v[204:207], v[60:75]\n"
"ds_read_b128 v[196:199], v5 offset:8192\n"
"ds_read_b64_tr_b16 v[200:201], v24 offset:32768\n"
"ds_read_b64_tr_b16 v[202:203], v24 offset:33280\n"
"v_max3_u32 v8, v8, v204, v205\n"
"v_max3_u32 v8, v8, v206, v207\n"
"s_waitcnt lgkmcnt(6)\n"
"v_mfma_f32_32x32x16_bf16 v[60:75], v[216:219], v[212:215], v[60:75]\n"
"ds_read_b128 v[204:207], v5 offset:9216\n"
"ds_read_b64_tr_b16 v[208:209], v24 offset:34816\n"
"ds_read_b64_tr_b16 v[210:211], v24 offset:35328\n"
"v_max3_u32 v8, v8, v212, v213\n"
"v_max3_u32 v8, v8, v214, v215\n"
"s_waitcnt lgkmcnt(6)\n"
"v_mfma_f32_32x32x16_bf16 v[60:75], v[224:227], v[220:223], v[60:75]\n"
"ds_read_b128 v[212:215], v5 offset:10240\n"
"ds_read_b64_tr_b16 v[216:217], v24 offset:36864\n"
"ds_read_b64_tr_b16 v[218:219], v24 offset:37376\n"
"v_max3_u32 v8, v8, v220, v221\n"
"v_max3_u32 v8, v8, v222, v223\n"
"s_waitcnt lgkmcnt(6)\n"
"v_mfma_f32_32x32x16_bf16 v[60:75], v[200:203], v[196:199], v[60:75]\n"
"ds_read_b128 v[220:223], v5 offset:11264\n"
"ds_read_b64_tr_b16 v[224:225], v24 offset:38912\n"
"ds_read_b64_tr_b16 v[226:227], v24 offset:39424\n"
"v_max3_u32 v8, v8, v196, v197\n"
"v_max3_u32 v8, v8, v198, v199\n"
"s_waitcnt lgkmcnt(6)\n"
"v_mfma_f32_32x32x16_bf16 v[60:75], v[208:211], v[204:207], v[60:75]\n"
"ds_read_b128 v[196:199], v5 offset:12288\n"
"ds_read_b64_tr_b16 v[200:201], v24 offset:24576\n"
"ds_read_b64_tr_b16 v[202:203], v24 offset:25088\n"
"v_max3_u32 v8, v8, v204, v205\n"
"v_max3_u32 v8, v8, v206, v207\n"
"s_waitcnt lgkmcnt(6)\n"
"v_mfma_f32_32x32x16_bf16 v[60:75], v[216:219], v[212:215], v[60:75]\n"
"ds_read_b128 v[204:207], v5 offset:13312\n"
"ds_read_b64_tr_b16 v[208:209], v24 offset:26624\n"
"ds_read_b64_tr_b16 v[210:211], v24 offset:27136\n"
"v_max3_u32 v8, v8, v212, v213\n"
"v_max3_u32 v8, v8, v214, v215\n"
"s_waitcnt lgkmcnt(6)\n"
"v_mfma_f32_32x32x16_bf16 v[60:75], v[224:227], v[220:223], v[60:75]\n"
"ds_read_b128 v[212:215], v5 offset:14336\n"
"ds_read_b64_tr_b16 v[216:217], v24 offset:28672\n"
"ds_read_b64_tr_b16 v[218:219], v24 offset:29184\n"
"v_max3_u32 v8, v8, v220, v221\n"
"v_max3_u32 v8, v8, v222, v223\n"
"s_waitcnt lgkmcnt(6)\n"
"v_mfma_f32_32x32x16_bf16 v[60:75], v[200:203], v[196:199], v[60:75]\n"
"ds_read_b128 v[220:223], v5 offset:15360\n"
"ds_read_b64_tr_b16 v[224:225], v24 offset:30720\n"
"ds_read_b64_tr_b16 v[226:227], v24 offset:31232\n"
"v_max3_u32 v8, v8, v196, v197\n"
"v_max3_u32 v8, v8, v198, v199\n"
"s_waitcnt lgkmcnt(6)\n"
"v_mfma_f32_32x32x16_bf16 v[60:75], v[208:211], v[204:207], v[60:75]\n"
"ds_read_b128 v[196:199], v5 offset:16384\n"
"ds_read_b64_tr_b16 v[200:201], v24 offset:16384\n"
"ds_read_b64_tr_b16 v[202:203], v24 offset:16896\n"
"v_max3_u32 v8, v8, v204, v205\n"
"v_max3_u32 v8, v8, v206, v207\n"
"s_waitcnt lgkmcnt(6)\n"
"v_mfma_f32_32x32x16_bf16 v[60:75], v[216:219], v[212:215], v[60:75]\n"
"ds_read_b128 v[204:207], v5 offset:17408\n"
"ds_read_b64_tr_b16 v[208:209], v24 offset:18432\n"
"ds_read_b64_tr_b16 v[210:211], v24 offset:18944\n"
"v_max3_u32 v8, v8, v212, v213\n"
"v_max3_u32 v8, v8, v214, v215\n"
"s_waitcnt lgkmcnt(6)\n"
"v_mfma_f32_32x32x16_bf16 v[60:75], v[224:227], v[220:223], v[60:75]\n"
"ds_read_b128 v[212:215], v5 offset:18432\n"
"ds_read_b64_tr_b16 v[216:217], v24 offset:20480\n"
"ds_read_b64_tr_b16 v[218:219], v24 offset:20992\n"
"v_max3_u32 v8, v8, v220, v221\n"
"v_max3_u32 v8, v8, v222, v223\n"
"s_waitcnt lgkmcnt(6)\n"
"v_mfma_f32_32x32x16_bf16 v[60:75], v[200:203], v[196:199], v[60:75]\n"
"ds_read_b128 v[220:223], v5 offset:19456\n"
"ds_read_b64_tr_b16 v[224:225], v24 offset:22528\n"
"ds_read_b64_tr_b16 v[226:227], v24 offset:23040\n"
"v_max3_u32 v8, v8, v196, v197\n"
"v_max3_u32 v8, v8, v198, v199\n"
"s_waitcnt lgkmcnt(6)\n"
"v_mfma_f32_32x32x16_bf16 v[60:75], v[208:211], v[204:207], v[60:75]\n"
"ds_read_b128 v[196:199], v5 offset:20480\n"
"ds_read_b64_tr_b16 v[200:201], v24 offset:8192\n"
"ds_read_b64_tr_b16 v[202:203], v24 offset:8704\n"
"v_max3_u32 v8, v8, v204, v205\n"
"v_max3_u32 v8, v8, v206, v207\n"
"s_waitcnt lgkmcnt(6)\n"
"v_mfma_f32_32x32x16_bf16 v[60:75], v[216:219], v[212:215], v[60:75]\n"
"ds_read_b128 v[204:207], v5 offset:21504\n"
"ds_read_b64_tr_b16 v[208:209], v24 offset:10240\n"
"ds_read_b64_tr_b16 v[210:211], v24 offset:10752\n"
"v_max3_u32 v8, v8, v212, v213\n"
"v_max3_u32 v8, v8, v214, v215\n"
"s_waitcnt lgkmcnt(6)\n"
"v_mfma_f32_32x32x16_bf16 v[60:75], v[224:227], v[220:223], v[60:75]\n"
"ds_read_b128 v[212:215], v5 offset:22528\n"
"ds_read_b64_tr_b16 v[216:217], v24 offset:12288\n"
"ds_read_b64_tr_b16 v[218:219], v24 offset:12800\n"
"v_max3_u32 v8, v8, v220, v221\n"
"v_max3_u32 v8, v8, v222, v223\n"
"s_waitcnt lgkmcnt(6)\n"
"v_mfma_f32_32x32x16_bf16 v[60:75], v[200:203], v[196:199], v[60:75]\n"
"ds_read_b128 v[220:223], v5 offset:23552\n"
"ds_read_b64_tr_b16 v[224:225], v24 offset:14336\n"
"ds_read_b64_tr_b16 v[226:227], v24 offset:14848\n"
"v_max3_u32 v8, v8, v196, v197\n"
"v_max3_u32 v8, v8, v198, v199\n"
"s_waitcnt lgkmcnt(6)\n"
"v_mfma_f32_32x32x16_bf16 v[60:75], v[208:211], v[204:207], v[60:75]\n"
"ds_read_b128 v[196:199], v5 offset:24576\n"
"ds_read_b64_tr_b16 v[200:201], v24 offset:0\n"
"ds_read_b64_tr_b16 v[202:203], v24 offset:512\n"
"v_max3_u32 v8, v8, v204, v205\n"
"v_max3_u32 v8, v8, v206, v207\n"
"s_waitcnt lgkmcnt(6)\n"
"v_mfma_f32_32x32x16_bf16 v[60:75], v[216:219], v[212:215], v[60:75]\n"
"ds_read_b128 v[204:207], v5 offset:25600\n"
"ds_read_b64_tr_b16 v[208:209], v24 offset:2048\n"
"ds_read_b64_tr_b16 v[210:211], v24 offset:2560\n"
"v_max3_u32 v8, v8, v212, v213\n"
"v_max3_u32 v8, v8, v214, v215\n"
"s_waitcnt lgkmcnt(6)\n"
"v_mfma_f32_32x32x16_bf16 v[60:75], v[224:227], v[220:223], v[60:75]\n"
"ds_read_b128 v[212:215], v5 offset:26624\n"
"ds_read_b64_tr_b16 v[216:217], v24 offset:4096\n"
"ds_read_b64_tr_b16 v[218:219], v24 offset:4608\n"
"v_max3_u32 v8, v8, v220, v221\n"
"v_max3_u32 v8, v8, v222, v223\n"
"s_waitcnt lgkmcnt(6)\n"
"v_mfma_f32_32x32x16_bf16 v[60:75], v[200:203], v[196:199], v[60:75]\n"
"ds_read_b128 v[220:223], v5 offset:27648\n"
"ds_read_b64_tr_b16 v[224:225], v24 offset:6144\n"
"ds_read_b64_tr_b16 v[226:227], v24 offset:6656\n"
"v_max3_u32 v8, v8, v196, v197\n"
"v_max3_u32 v8, v8, v198, v199\n"
"s_waitcnt lgkmcnt(6)\n"
"v_mfma_f32_32x32x16_bf16 v[60:75], v[208:211], v[204:207], v[60:75]\n"
"v_max3_u32 v8, v8, v204, v205\n"
"v_max3_u32 v8, v8, v206, v207\n"
"s_waitcnt lgkmcnt(3)\n"
"v_mfma_f32_32x32x16_bf16 v[60:75], v[216:219], v[212:215], v[60:75]\n"
"v_max3_u32 v8, v8, v212, v213\n"
"v_max3_u32 v8, v8, v214, v215\n"
"s_waitcnt lgkmcnt(0)\n"
"v_mfma_f32_32x32x16_bf16 v[60:75], v[224:227], v[220:223], v[60:75]\n"
"v_max3_u32 v8, v8, v220, v221\n"
"v_max3_u32 v8, v8, v222, v223\n"
"v_cmp_eq_u32 vcc, -1, v8\n"
"s_nop 7\n"
"s_cbranch_vccnz .Lfused_fwd_aretry\n"
".Lfused_fwd_aretry_r:\n"
"v_cvt_pk_bf16_f32 v196, v60, v61\n"
"v_cvt_pk_bf16_f32 v197, v62, v63\n"
"v_cvt_pk_bf16_f32 v198, v64, v65\n"
"v_cvt_pk_bf16_f32 v199, v66, v67\n"
"v_cvt_pk_bf16_f32 v200, v68, v69\n"
"v_cvt_pk_bf16_f32 v201, v70, v71\n"
"v_cvt_pk_bf16_f32 v202, v72, v73\n"
"v_cvt_pk_bf16_f32 v203, v74, v75\n"
"v_permlane32_swap_b32 v196, v198\n"
"v_permlane32_swap_b32 v197, v199\n"
"v_permlane32_swap_b32 v200, v202\n"
"v_permlane32_swap_b32 v201, v203\n"
".Lfused_fwd_final_l:\n"
"v_lshrrev_b32 v5, 4, v1\n"
"v_and_b32 v6, 15, v1\n"
"v_lshlrev_b32 v6, 4, v6\n"
"v_lshl_add_u32 v5, v5, 9, v6\n"
"v_add_u32 v6, 155808, v5\n"
"v_add_u32 v5, 57456, v5\n"
"s_barrier\n"
"ds_read_b128 v[76:79], v5 offset:0\n"
"ds_read_b128 v[80:83], v5 offset:256\n"
"ds_read_b128 v[84:87], v5 offset:2048\n"
"ds_read_b128 v[88:91], v5 offset:2304\n"
"ds_read_b128 v[92:95], v5 offset:4096\n"
"ds_read_b128 v[96:99], v5 offset:4352\n"
"ds_read_b128 v[100:103], v5 offset:6144\n"
"ds_read_b128 v[104:107], v5 offset:6400\n"
"ds_read_b128 v[108:111], v5 offset:8192\n"
"ds_read_b128 v[112:115], v5 offset:8448\n"
"ds_read_b128 v[116:119], v5 offset:10240\n"
"ds_read_b128 v[120:123], v5 offset:10496\n"
"buffer_store_dwordx4 v[196:199], v59, s[32:35], s12 offen\n"
"buffer_store_dwordx4 v[200:203], v59, s[32:35], s12 offen offset:32\n"
"s_waitcnt lgkmcnt(10)\n"
"v_mfma_f32_16x16x32_bf16 v[32:35], v[140:143], v[76:79], 0\n"
"v_mfma_f32_16x16x32_bf16 v[36:39], v[140:143], v[80:83], 0\n"
"ds_read_b128 v[124:127], v5 offset:12288\n"
"ds_read_b128 v[128:131], v5 offset:12544\n"
"s_waitcnt lgkmcnt(10)\n"
"v_mfma_f32_16x16x32_bf16 v[32:35], v[144:147], v[84:87], v[32:35]\n"
"v_mfma_f32_16x16x32_bf16 v[36:39], v[144:147], v[88:91], v[36:39]\n"
"ds_read_b128 v[76:79], v5 offset:14336\n"
"ds_read_b128 v[80:83], v5 offset:14592\n"
"s_waitcnt lgkmcnt(10)\n"
"v_mfma_f32_16x16x32_bf16 v[32:35], v[148:151], v[92:95], v[32:35]\n"
"v_mfma_f32_16x16x32_bf16 v[36:39], v[148:151], v[96:99], v[36:39]\n"
"ds_read_b128 v[84:87], v5 offset:16384\n"
"ds_read_b128 v[88:91], v5 offset:16640\n"
"s_waitcnt lgkmcnt(10)\n"
"v_mfma_f32_16x16x32_bf16 v[32:35], v[152:155], v[100:103], v[32:35]\n"
"v_mfma_f32_16x16x32_bf16 v[36:39], v[152:155], v[104:107], v[36:39]\n"
"ds_read_b128 v[92:95], v5 offset:18432\n"
"ds_read_b128 v[96:99], v5 offset:18688\n"
"s_waitcnt lgkmcnt(10)\n"
"v_mfma_f32_16x16x32_bf16 v[32:35], v[156:159], v[108:111], v[32:35]\n"
"v_mfma_f32_16x16x32_bf16 v[36:39], v[156:159], v[112:115], v[36:39]\n"
"ds_read_b128 v[100:103], v5 offset:20480\n"
"ds_read_b128 v[104:107], v5 offset:20736\n"
"s_waitcnt lgkmcnt(10)\n"
"v_mfma_f32_16x16x32_bf16 v[32:35], v[160:163], v[116:119], v[32:35]\n"
"v_mfma_f32_16x16x32_bf16 v[36:39], v[160:163], v[120:123], v[36:39]\n"
"ds_read_b128 v[108:111], v5 offset:22528\n"
"ds_read_b128 v[112:115], v5 offset:22784\n"
"s_waitcnt lgkmcnt(10)\n"
"v_mfma_f32_16x16x32_bf16 v[32:35], v[164:167], v[124:127], v[32:35]\n"
"v_mfma_f32_16x16x32_bf16 v[36:39], v[164:167], v[128:131], v[36:39]\n"
"ds_read_b128 v[116:119], v6 offset:0\n"
"ds_read_b128 v[120:123], v6 offset:256\n"
"s_waitcnt lgkmcnt(10)\n"
"v_mfma_f32_16x16x32_bf16 v[32:35], v[168:171], v[76:79], v[32:35]\n"
"v_mfma_f32_16x16x32_bf16 v[36:39], v[168:171], v[80:83], v[36:39]\n"
"ds_read_b128 v[124:127], v6 offset:2048\n"
"ds_read_b128 v[128:131], v6 offset:2304\n"
"s_waitcnt lgkmcnt(10)\n"
"v_mfma_f32_16x16x32_bf16 v[32:35], v[172:175], v[84:87], v[32:35]\n"
"v_mfma_f32_16x16x32_bf16 v[36:39], v[172:175], v[88:91], v[36:39]\n"
"s_waitcnt lgkmcnt(8)\n"
"v_mfma_f32_16x16x32_bf16 v[32:35], v[176:179], v[92:95], v[32:35]\n"
"v_mfma_f32_16x16x32_bf16 v[36:39], v[176:179], v[96:99], v[36:39]\n"
"s_waitcnt lgkmcnt(6)\n"
"v_mfma_f32_16x16x32_bf16 v[32:35], v[180:183], v[100:103], v[32:35]\n"
"v_mfma_f32_16x16x32_bf16 v[36:39], v[180:183], v[104:107], v[36:39]\n"
"s_waitcnt lgkmcnt(4)\n"
"v_mfma_f32_16x16x32_bf16 v[32:35], v[184:187], v[108:111], v[32:35]\n"
"v_mfma_f32_16x16x32_bf16 v[36:39], v[184:187], v[112:115], v[36:39]\n"
"s_waitcnt lgkmcnt(2)\n"
"v_mfma_f32_16x16x32_bf16 v[32:35], v[188:191], v[116:119], v[32:35]\n"
"v_mfma_f32_16x16x32_bf16 v[36:39], v[188:191], v[120:123], v[36:39]\n"
"s_waitcnt lgkmcnt(0)\n"
"v_mfma_f32_16x16x32_bf16 v[32:35], v[192:195], v[124:127], v[32:35]\n"
"v_mfma_f32_16x16x32_bf16 v[36:39], v[192:195], v[128:131], v[36:39]\n"
"s_nop 6\n"
"v_cvt_pk_bf16_f32 v40, v32, v33\n"
"v_cvt_pk_bf16_f32 v42, v36, v37\n"
"v_cvt_pk_bf16_f32 v41, v34, v35\n"
"v_cvt_pk_bf16_f32 v43, v38, v39\n"
"v_permlane16_swap_b32 v40, v42\n"
"s_nop 0\n"
"v_permlane16_swap_b32 v41, v43\n"
"buffer_store_dwordx4 v[40:43], v58, s[32:35], 0 offen\n"
"s_endpgm\n"
".Lfused_fwd_pro_c:\n"
"s_cmp_eq_u32 s43, 0\n"
"s_cselect_b32 s4, -1, 0\n"
"s_mov_b32 s5, -1\n"
"s_mov_b32 s6, -1\n"
"s_cselect_b32 s7, 0, -1\n"
"s_cselect_b32 s8, 0x0fffffff, -16\n"
"s_mov_b32 s9, s8\n"
"s_and_b32 s10, s61, 0xff\n"
"s_mul_i32 s10, s10, 57344\n"
"s_add_u32 s10, s10, 12582912\n"
"s_lshr_b32 s11, s61, 8\n"
"s_mul_i32 s11, s11, 28672\n"
"s_add_u32 s11, s11, 14680064\n"
"s_cmp_eq_u32 s43, 0\n"
"s_cselect_b32 s50, 1024, 8\n"
"s_and_b64 vcc, s[4:5], s[6:7]\n"
"v_mov_b32 v5, 2130706432\n"
"v_add_u32 v6, s50, v4\n"
"v_cndmask_b32 v56, v5, v4, s[4:5]\n"
"v_cndmask_b32 v22, v5, v4, s[6:7]\n"
"v_cndmask_b32 v11, v6, v5, vcc\n"
"s_add_u32 s14, s58, 6291456\n"
"s_add_u32 s15, s14, 28672\n"
"s_add_u32 s16, s14, 57344\n"
"s_cmp_eq_u32 s41, 21\n"
"s_cselect_b32 s15, 2130706432, s15\n"
"s_cselect_b32 s16, 2130706432, s16\n"
"s_lshl_b32 s50, s46, 11\n"
"s_add_u32 s50, s50, 82080\n"
"v_add_u32 v5, s50, v4\n"
"v_add_u32 v6, 65664, v4\n"
"s_barrier\n"
"ds_read_b128 v[32:35], v5\n"
"ds_read_b128 v[36:39], v5 offset:1024\n"
"ds_read_b128 v[40:43], v13 offset:8208\n"
"ds_read_b128 v[44:47], v13 offset:9232\n"
"s_waitcnt lgkmcnt(2)\n"
"v_cndmask_b32 v32, 0, v32, s[4:5]\n"
"v_cndmask_b32 v33, 0, v33, s[4:5]\n"
"v_cndmask_b32 v38, 0, v38, s[6:7]\n"
"v_cndmask_b32 v39, 0, v39, s[6:7]\n"
"s_waitcnt lgkmcnt(0)\n"
"v_mfma_f32_32x32x16_bf16 v[108:123], v[40:43], v[32:35], 0\n"
"v_mfma_f32_32x32x16_bf16 v[108:123], v[44:47], v[36:39], v[108:123]\n"
"ds_read_b128 v[48:51], v15 offset:8208\n"
"s_nop 10\n"
"v_max3_f32 v7, v108, v109, v110\n"
"v_max3_f32 v8, v111, v112, v113\n"
"v_max3_f32 v7, v7, v114, v115\n"
"v_max3_f32 v8, v8, v116, v117\n"
"v_max3_f32 v7, v7, v118, v119\n"
"v_max3_f32 v8, v8, v120, v121\n"
"v_max3_f32 v7, v7, v122, v123\n"
"v_max_f32 v7, v7, v8\n"
"v_mov_b32 v8, v7\n"
"s_nop 1\n"
"v_permlane32_swap_b32 v7, v8\n"
"v_max_f32 v7, v7, v8\n"
"v_sub_f32 v60, 0xc2c00000, v7\n"
"v_mov_b32 v61, v60\n"
"v_mov_b32 v62, v60\n"
"v_mov_b32 v63, v60\n"
"v_mov_b32 v64, v60\n"
"v_mov_b32 v65, v60\n"
"v_mov_b32 v66, v60\n"
"v_mov_b32 v67, v60\n"
"v_mov_b32 v68, v60\n"
"v_mov_b32 v69, v60\n"
"v_mov_b32 v70, v60\n"
"v_mov_b32 v71, v60\n"
"v_mov_b32 v72, v60\n"
"v_mov_b32 v73, v60\n"
"v_mov_b32 v74, v60\n"
"v_mov_b32 v75, v60\n"
"s_waitcnt lgkmcnt(0)\n"
"s_setprio 3\n"
"v_mfma_f32_32x32x16_bf16 v[92:107], v[40:43], v[32:35], v[60:75]\n"
"ds_read_b128 v[40:43], v13 offset:16416\n"
"v_mfma_f32_32x32x16_bf16 v[92:107], v[44:47], v[36:39], v[92:107]\n"
"ds_read_b128 v[44:47], v13 offset:17440\n"
"s_waitcnt lgkmcnt(0)\n"
"s_barrier\n"
"s_nop 7\n"
"v_mfma_f32_32x32x16_bf16 v[108:123], v[40:43], v[32:35], v[60:75]\n"
"s_add_u32 m0, s48, 32832\n"
"ds_read_b128 v[40:43], v12 offset:0\n"
"buffer_load_dwordx4 v10, s[20:23], s70 offen sc1 lds\n"
"v_exp_f32 v92, v92\n"
"v_exp_f32 v93, v93\n"
"v_exp_f32 v94, v94\n"
"v_exp_f32 v95, v95\n"
"s_add_u32 m0, s48, 41040\n"
"ds_read_b128 v[52:55], v17 offset:8208\n"
"buffer_load_dwordx4 v10, s[20:23], s71 offen sc1 lds\n"
"s_setprio 2\n"
"v_exp_f32 v96, v96\n"
"v_exp_f32 v97, v97\n"
"v_exp_f32 v98, v98\n"
"v_exp_f32 v99, v99\n"
"v_mfma_f32_32x32x16_bf16 v[108:123], v[44:47], v[36:39], v[108:123]\n"
"ds_read_b128 v[44:47], v12 offset:1024\n"
"buffer_store_dwordx4 v[220:223], v206, s[20:23], s14 offen sc1\n"
"v_exp_f32 v100, v100\n"
"v_exp_f32 v101, v101\n"
"v_exp_f32 v102, v102\n"
"v_exp_f32 v103, v103\n"
"v_smfmac_f32_32x32x32_bf16 v[76:91], v[48:51], v[92:99], v9\n"
"ds_read_b128 v[48:51], v15 offset:16416\n"
"s_waitcnt lgkmcnt(2)\n"
"s_setprio 1\n"
"v_exp_f32 v104, v104\n"
"v_exp_f32 v105, v105\n"
"v_exp_f32 v106, v106\n"
"v_exp_f32 v107, v107\n"
"v_mfma_f32_32x32x16_bf16 v[124:139], v[40:43], v[32:35], v[60:75]\n"
"s_add_u32 m0, s48, 49248\n"
"ds_read_b128 v[40:43], v12 offset:8208\n"
"buffer_load_dwordx4 v10, s[20:23], s72 offen sc1 lds\n"
"v_exp_f32 v108, v108\n"
"v_exp_f32 v109, v109\n"
"v_exp_f32 v110, v110\n"
"v_exp_f32 v111, v111\n"
"v_smfmac_f32_32x32x32_bf16 v[76:91], v[52:55], v[100:107], v9\n"
"s_add_u32 m0, s48, 57456\n"
"ds_read_b128 v[52:55], v17 offset:16416\n"
"buffer_load_dwordx4 v10, s[20:23], s73 offen sc1 lds\n"
"s_waitcnt lgkmcnt(2)\n"
"s_setprio 0\n"
"v_exp_f32 v112, v112\n"
"v_exp_f32 v113, v113\n"
"v_exp_f32 v114, v114\n"
"v_exp_f32 v115, v115\n"
"v_mfma_f32_32x32x16_bf16 v[124:139], v[44:47], v[36:39], v[124:139]\n"
"ds_read_b128 v[44:47], v12 offset:9232\n"
"buffer_store_dwordx4 v[224:227], v207, s[20:23], s14 offen sc1\n"
"v_exp_f32 v116, v116\n"
"v_exp_f32 v117, v117\n"
"v_exp_f32 v118, v118\n"
"v_exp_f32 v119, v119\n"
"v_smfmac_f32_32x32x32_bf16 v[76:91], v[48:51], v[108:115], v9\n"
"ds_read_b128 v[48:51], v14 offset:0\n"
"s_waitcnt lgkmcnt(2)\n"
"s_barrier\n"
"s_setprio 3\n"
"v_exp_f32 v120, v120\n"
"v_exp_f32 v121, v121\n"
"v_exp_f32 v122, v122\n"
"v_exp_f32 v123, v123\n"
"v_mfma_f32_32x32x16_bf16 v[92:107], v[40:43], v[32:35], v[60:75]\n"
"ds_read_b128 v[40:43], v12 offset:16416\n"
"v_exp_f32 v124, v124\n"
"v_exp_f32 v125, v125\n"
"v_exp_f32 v126, v126\n"
"v_exp_f32 v127, v127\n"
"v_smfmac_f32_32x32x32_bf16 v[76:91], v[52:55], v[116:123], v9\n"
"s_add_u32 m0, s48, 65664\n"
"ds_read_b128 v[52:55], v16 offset:0\n"
"buffer_load_dwordx4 v10, s[20:23], s74 offen sc1 lds\n"
"s_waitcnt lgkmcnt(2)\n"
"s_setprio 2\n"
"v_exp_f32 v128, v128\n"
"v_exp_f32 v129, v129\n"
"v_exp_f32 v130, v130\n"
"v_exp_f32 v131, v131\n"
"v_mfma_f32_32x32x16_bf16 v[92:107], v[44:47], v[36:39], v[92:107]\n"
"ds_read_b128 v[44:47], v12 offset:17440\n"
"buffer_store_dwordx4 v[228:231], v206, s[20:23], s15 offen sc1\n"
"v_exp_f32 v132, v132\n"
"v_exp_f32 v133, v133\n"
"v_exp_f32 v134, v134\n"
"v_exp_f32 v135, v135\n"
"v_smfmac_f32_32x32x32_bf16 v[76:91], v[48:51], v[124:131], v9\n"
"ds_read_b128 v[48:51], v14 offset:8208\n"
"s_waitcnt lgkmcnt(2)\n"
"s_setprio 1\n"
"v_exp_f32 v136, v136\n"
"v_exp_f32 v137, v137\n"
"v_exp_f32 v138, v138\n"
"v_exp_f32 v139, v139\n"
"v_mfma_f32_32x32x16_bf16 v[108:123], v[40:43], v[32:35], v[60:75]\n"
"ds_read_b128 v[40:43], v12 offset:24624\n"
"v_exp_f32 v92, v92\n"
"v_exp_f32 v93, v93\n"
"v_exp_f32 v94, v94\n"
"v_exp_f32 v95, v95\n"
"v_smfmac_f32_32x32x32_bf16 v[76:91], v[52:55], v[132:139], v9\n"
"s_add_u32 m0, s48, 73872\n"
"ds_read_b128 v[52:55], v16 offset:8208\n"
"buffer_load_dwordx4 v10, s[20:23], s75 offen sc1 lds\n"
"s_waitcnt lgkmcnt(2)\n"
"s_setprio 0\n"
"v_exp_f32 v96, v96\n"
"v_exp_f32 v97, v97\n"
"v_exp_f32 v98, v98\n"
"v_exp_f32 v99, v99\n"
"v_mfma_f32_32x32x16_bf16 v[108:123], v[44:47], v[36:39], v[108:123]\n"
"ds_read_b128 v[44:47], v12 offset:25648\n"
"buffer_store_dwordx4 v[232:235], v207, s[20:23], s15 offen sc1\n"
"v_exp_f32 v100, v100\n"
"v_exp_f32 v101, v101\n"
"v_exp_f32 v102, v102\n"
"v_exp_f32 v103, v103\n"
"v_smfmac_f32_32x32x32_bf16 v[76:91], v[48:51], v[92:99], v9\n"
"ds_read_b128 v[48:51], v14 offset:16416\n"
"s_waitcnt vmcnt(8) lgkmcnt(2)\n"
"s_barrier\n"
"s_setprio 3\n"
"v_exp_f32 v104, v104\n"
"v_exp_f32 v105, v105\n"
"v_exp_f32 v106, v106\n"
"v_exp_f32 v107, v107\n"
"v_mfma_f32_32x32x16_bf16 v[124:139], v[40:43], v[32:35], v[60:75]\n"
"ds_read_b128 v[40:43], v12 offset:32832\n"
"v_exp_f32 v108, v108\n"
"v_exp_f32 v109, v109\n"
"v_exp_f32 v110, v110\n"
"v_exp_f32 v111, v111\n"
"v_smfmac_f32_32x32x32_bf16 v[76:91], v[52:55], v[100:107], v9\n"
"s_add_u32 m0, s48, 0\n"
"ds_read_b128 v[52:55], v16 offset:16416\n"
"buffer_load_dwordx4 v10, s[20:23], s76 offen sc1 lds\n"
"s_waitcnt lgkmcnt(2)\n"
"s_setprio 2\n"
"v_exp_f32 v112, v112\n"
"v_exp_f32 v113, v113\n"
"v_exp_f32 v114, v114\n"
"v_exp_f32 v115, v115\n"
"v_mfma_f32_32x32x16_bf16 v[124:139], v[44:47], v[36:39], v[124:139]\n"
"ds_read_b128 v[44:47], v12 offset:33856\n"
"buffer_store_dwordx4 v[236:239], v206, s[20:23], s16 offen sc1\n"
"v_exp_f32 v116, v116\n"
"v_exp_f32 v117, v117\n"
"v_exp_f32 v118, v118\n"
"v_exp_f32 v119, v119\n"
"v_smfmac_f32_32x32x32_bf16 v[76:91], v[48:51], v[108:115], v9\n"
"ds_read_b128 v[48:51], v14 offset:24624\n"
"s_waitcnt lgkmcnt(2)\n"
"s_setprio 1\n"
"v_exp_f32 v120, v120\n"
"v_exp_f32 v121, v121\n"
"v_exp_f32 v122, v122\n"
"v_exp_f32 v123, v123\n"
"v_mfma_f32_32x32x16_bf16 v[92:107], v[40:43], v[32:35], v[60:75]\n"
"ds_read_b128 v[40:43], v12 offset:41040\n"
"v_exp_f32 v124, v124\n"
"v_exp_f32 v125, v125\n"
"v_exp_f32 v126, v126\n"
"v_exp_f32 v127, v127\n"
"v_smfmac_f32_32x32x32_bf16 v[76:91], v[52:55], v[116:123], v9\n"
"s_add_u32 m0, s48, 8208\n"
"ds_read_b128 v[52:55], v16 offset:24624\n"
"buffer_load_dwordx4 v10, s[20:23], s77 offen sc1 lds\n"
"s_waitcnt lgkmcnt(2)\n"
"s_setprio 0\n"
"v_exp_f32 v128, v128\n"
"v_exp_f32 v129, v129\n"
"v_exp_f32 v130, v130\n"
"v_exp_f32 v131, v131\n"
"v_mfma_f32_32x32x16_bf16 v[92:107], v[44:47], v[36:39], v[92:107]\n"
"ds_read_b128 v[44:47], v12 offset:42064\n"
"buffer_store_dwordx4 v[240:243], v207, s[20:23], s16 offen sc1\n"
"v_exp_f32 v132, v132\n"
"v_exp_f32 v133, v133\n"
"v_exp_f32 v134, v134\n"
"v_exp_f32 v135, v135\n"
"v_smfmac_f32_32x32x32_bf16 v[76:91], v[48:51], v[124:131], v9\n"
"ds_read_b128 v[48:51], v14 offset:32832\n"
"s_waitcnt vmcnt(9) lgkmcnt(2)\n"
"s_barrier\n"
"s_setprio 3\n"
"v_exp_f32 v136, v136\n"
"v_exp_f32 v137, v137\n"
"v_exp_f32 v138, v138\n"
"v_exp_f32 v139, v139\n"
"v_mfma_f32_32x32x16_bf16 v[108:123], v[40:43], v[32:35], v[60:75]\n"
"ds_read_b128 v[40:43], v12 offset:49248\n"
"v_exp_f32 v92, v92\n"
"v_exp_f32 v93, v93\n"
"v_exp_f32 v94, v94\n"
"v_exp_f32 v95, v95\n"
"v_smfmac_f32_32x32x32_bf16 v[76:91], v[52:55], v[132:139], v9\n"
"s_add_u32 m0, s48, 16416\n"
"ds_read_b128 v[52:55], v16 offset:32832\n"
"buffer_load_dwordx4 v10, s[20:23], s78 offen sc1 lds\n"
"s_waitcnt lgkmcnt(2)\n"
"s_setprio 2\n"
"v_exp_f32 v96, v96\n"
"v_exp_f32 v97, v97\n"
"v_exp_f32 v98, v98\n"
"v_exp_f32 v99, v99\n"
"v_mfma_f32_32x32x16_bf16 v[108:123], v[44:47], v[36:39], v[108:123]\n"
"s_add_u32 m0, s48, 147616\n"
"s_mov_b32 s17, 0\n"
"ds_read_b128 v[44:47], v12 offset:50272\n"
"buffer_load_dwordx4 v18, s[24:27], s17 offen lds\n"
"v_exp_f32 v100, v100\n"
"v_exp_f32 v101, v101\n"
"v_exp_f32 v102, v102\n"
"v_exp_f32 v103, v103\n"
"v_smfmac_f32_32x32x32_bf16 v[76:91], v[48:51], v[92:99], v9\n"
"ds_read_b128 v[48:51], v14 offset:41040\n"
"s_waitcnt lgkmcnt(2)\n"
"s_setprio 1\n"
"v_exp_f32 v104, v104\n"
"v_exp_f32 v105, v105\n"
"v_exp_f32 v106, v106\n"
"v_exp_f32 v107, v107\n"
"v_mfma_f32_32x32x16_bf16 v[124:139], v[40:43], v[32:35], v[60:75]\n"
"ds_read_b128 v[40:43], v13 offset:0\n"
"v_exp_f32 v108, v108\n"
"v_exp_f32 v109, v109\n"
"v_exp_f32 v110, v110\n"
"v_exp_f32 v111, v111\n"
"v_smfmac_f32_32x32x32_bf16 v[76:91], v[52:55], v[100:107], v9\n"
"s_add_u32 m0, s48, 24624\n"
"ds_read_b128 v[52:55], v16 offset:41040\n"
"buffer_load_dwordx4 v10, s[20:23], s79 offen sc1 lds\n"
"s_waitcnt lgkmcnt(2)\n"
"s_setprio 0\n"
"v_exp_f32 v112, v112\n"
"v_exp_f32 v113, v113\n"
"v_exp_f32 v114, v114\n"
"v_exp_f32 v115, v115\n"
"v_mfma_f32_32x32x16_bf16 v[124:139], v[44:47], v[36:39], v[124:139]\n"
"s_add_u32 m0, s48, 139424\n"
"s_mov_b32 s17, 28672\n"
"ds_read_b128 v[44:47], v13 offset:1024\n"
"buffer_load_dwordx4 v18, s[24:27], s17 offen lds\n"
"v_exp_f32 v116, v116\n"
"v_exp_f32 v117, v117\n"
"v_exp_f32 v118, v118\n"
"v_exp_f32 v119, v119\n"
"v_smfmac_f32_32x32x32_bf16 v[76:91], v[48:51], v[108:115], v9\n"
"ds_read_b128 v[48:51], v14 offset:49248\n"
"s_waitcnt vmcnt(4) lgkmcnt(2)\n"
"s_barrier\n"
"s_setprio 3\n"
"v_exp_f32 v120, v120\n"
"v_exp_f32 v121, v121\n"
"v_exp_f32 v122, v122\n"
"v_exp_f32 v123, v123\n"
"v_mfma_f32_32x32x16_bf16 v[92:107], v[40:43], v[32:35], v[60:75]\n"
"ds_read_b128 v[40:43], v13 offset:8208\n"
"s_add_u32 s14, s59, 65536\n"
"s_cmp_eq_u32 s46, 0\n"
"s_cselect_b32 s14, s14, 2130706432\n"
"v_mov_b32 v24, s36\n"
"v_mov_b32 v25, s37\n"
"s_mov_b64 exec, 1\n"
"buffer_store_dwordx2 v[24:25], off, s[20:23], s14 sc1\n"
"s_mov_b64 exec, -1\n"
"v_exp_f32 v124, v124\n"
"v_exp_f32 v125, v125\n"
"v_exp_f32 v126, v126\n"
"v_exp_f32 v127, v127\n"
"v_smfmac_f32_32x32x32_bf16 v[76:91], v[52:55], v[116:123], v9\n"
"s_add_u32 m0, s48, 32832\n"
"ds_read_b128 v[52:55], v16 offset:49248\n"
"buffer_load_dwordx4 v10, s[20:23], s80 offen sc1 lds\n"
"s_waitcnt lgkmcnt(2)\n"
"s_setprio 2\n"
"v_exp_f32 v128, v128\n"
"v_exp_f32 v129, v129\n"
"v_exp_f32 v130, v130\n"
"v_exp_f32 v131, v131\n"
"v_mfma_f32_32x32x16_bf16 v[92:107], v[44:47], v[36:39], v[92:107]\n"
"s_add_u32 m0, s48, 131232\n"
"s_mov_b32 s17, 57344\n"
"ds_read_b128 v[44:47], v13 offset:9232\n"
"buffer_load_dwordx4 v18, s[24:27], s17 offen lds\n"
"v_exp_f32 v132, v132\n"
"v_exp_f32 v133, v133\n"
"v_exp_f32 v134, v134\n"
"v_exp_f32 v135, v135\n"
"v_smfmac_f32_32x32x32_bf16 v[76:91], v[48:51], v[124:131], v9\n"
"ds_read_b128 v[48:51], v15 offset:0\n"
"s_waitcnt lgkmcnt(2)\n"
"s_setprio 1\n"
"v_exp_f32 v136, v136\n"
"v_exp_f32 v137, v137\n"
"v_exp_f32 v138, v138\n"
"v_exp_f32 v139, v139\n"
"v_mfma_f32_32x32x16_bf16 v[108:123], v[40:43], v[32:35], v[60:75]\n"
"ds_read_b128 v[40:43], v13 offset:16416\n"
"v_exp_f32 v92, v92\n"
"v_exp_f32 v93, v93\n"
"v_exp_f32 v94, v94\n"
"v_exp_f32 v95, v95\n"
"v_smfmac_f32_32x32x32_bf16 v[76:91], v[52:55], v[132:139], v9\n"
"s_add_u32 m0, s48, 41040\n"
"ds_read_b128 v[52:55], v17 offset:0\n"
"buffer_load_dwordx4 v10, s[20:23], s81 offen sc1 lds\n"
"s_waitcnt lgkmcnt(2)\n"
"s_setprio 0\n"
"v_exp_f32 v96, v96\n"
"v_exp_f32 v97, v97\n"
"v_exp_f32 v98, v98\n"
"v_exp_f32 v99, v99\n"
"v_mfma_f32_32x32x16_bf16 v[108:123], v[44:47], v[36:39], v[108:123]\n"
"s_add_u32 m0, s48, 123040\n"
"s_mov_b32 s17, 86016\n"
"ds_read_b128 v[44:47], v13 offset:17440\n"
"buffer_load_dwordx4 v18, s[24:27], s17 offen lds\n"
"v_exp_f32 v100, v100\n"
"v_exp_f32 v101, v101\n"
"v_exp_f32 v102, v102\n"
"v_exp_f32 v103, v103\n"
"v_smfmac_f32_32x32x32_bf16 v[76:91], v[48:51], v[92:99], v9\n"
"ds_read_b128 v[48:51], v15 offset:8208\n"
"s_waitcnt vmcnt(5) lgkmcnt(2)\n"
"s_barrier\n"
"s_setprio 3\n"
"v_exp_f32 v104, v104\n"
"v_exp_f32 v105, v105\n"
"v_exp_f32 v106, v106\n"
"v_exp_f32 v107, v107\n"
"v_mfma_f32_32x32x16_bf16 v[124:139], v[40:43], v[32:35], v[60:75]\n"
"ds_read_b128 v[40:43], v12 offset:0\n"
"ds_read_b64_tr_b16 v[140:141], v21 offset:16384\n"
"v_exp_f32 v108, v108\n"
"v_exp_f32 v109, v109\n"
"v_exp_f32 v110, v110\n"
"v_exp_f32 v111, v111\n"
"v_smfmac_f32_32x32x32_bf16 v[76:91], v[52:55], v[100:107], v9\n"
"s_add_u32 m0, s48, 49248\n"
"ds_read_b128 v[52:55], v17 offset:8208\n"
"buffer_load_dwordx4 v10, s[20:23], s82 offen sc1 lds\n"
"ds_read_b64_tr_b16 v[142:143], v21 offset:17408\n"
"s_waitcnt lgkmcnt(4)\n"
"s_setprio 2\n"
"v_exp_f32 v112, v112\n"
"v_exp_f32 v113, v113\n"
"v_exp_f32 v114, v114\n"
"v_exp_f32 v115, v115\n"
"v_mfma_f32_32x32x16_bf16 v[124:139], v[44:47], v[36:39], v[124:139]\n"
"s_add_u32 m0, s48, 114848\n"
"s_mov_b32 s17, 114688\n"
"ds_read_b128 v[44:47], v12 offset:1024\n"
"buffer_load_dwordx4 v18, s[24:27], s17 offen lds\n"
"v_exp_f32 v116, v116\n"
"v_exp_f32 v117, v117\n"
"v_exp_f32 v118, v118\n"
"v_exp_f32 v119, v119\n"
"v_smfmac_f32_32x32x32_bf16 v[76:91], v[48:51], v[108:115], v9\n"
"ds_read_b128 v[48:51], v15 offset:16416\n"
"s_waitcnt lgkmcnt(3)\n"
"s_setprio 1\n"
"v_exp_f32 v120, v120\n"
"v_exp_f32 v121, v121\n"
"v_exp_f32 v122, v122\n"
"v_exp_f32 v123, v123\n"
"v_mfma_f32_32x32x16_bf16 v[92:107], v[40:43], v[32:35], v[60:75]\n"
"ds_read_b128 v[40:43], v12 offset:8208\n"
"ds_read_b64_tr_b16 v[144:145], v21 offset:8192\n"
"v_exp_f32 v124, v124\n"
"v_exp_f32 v125, v125\n"
"v_exp_f32 v126, v126\n"
"v_exp_f32 v127, v127\n"
"v_smfmac_f32_32x32x32_bf16 v[76:91], v[52:55], v[116:123], v9\n"
"s_add_u32 m0, s48, 57456\n"
"ds_read_b128 v[52:55], v17 offset:16416\n"
"buffer_load_dwordx4 v10, s[20:23], s83 offen sc1 lds\n"
"ds_read_b64_tr_b16 v[146:147], v21 offset:9216\n"
"s_waitcnt lgkmcnt(4)\n"
"s_setprio 0\n"
"v_exp_f32 v128, v128\n"
"v_exp_f32 v129, v129\n"
"v_exp_f32 v130, v130\n"
"v_exp_f32 v131, v131\n"
"v_mfma_f32_32x32x16_bf16 v[92:107], v[44:47], v[36:39], v[92:107]\n"
"s_add_u32 m0, s48, 106656\n"
"s_mov_b32 s17, 143360\n"
"ds_read_b128 v[44:47], v12 offset:9232\n"
"buffer_load_dwordx4 v18, s[24:27], s17 offen lds\n"
"v_exp_f32 v132, v132\n"
"v_exp_f32 v133, v133\n"
"v_exp_f32 v134, v134\n"
"v_exp_f32 v135, v135\n"
"v_smfmac_f32_32x32x32_bf16 v[76:91], v[48:51], v[124:131], v9\n"
"ds_read_b128 v[48:51], v14 offset:0\n"
"s_waitcnt vmcnt(4) lgkmcnt(2)\n"
"s_barrier\n"
"s_setprio 3\n"
"v_exp_f32 v136, v136\n"
"v_exp_f32 v137, v137\n"
"v_exp_f32 v138, v138\n"
"v_exp_f32 v139, v139\n"
"v_mfma_f32_32x32x16_bf16 v[108:123], v[40:43], v[32:35], v[60:75]\n"
"ds_read_b128 v[40:43], v12 offset:16416\n"
"ds_read_b64_tr_b16 v[148:149], v21 offset:0\n"
"v_exp_f32 v92, v92\n"
"v_exp_f32 v93, v93\n"
"v_exp_f32 v94, v94\n"
"v_exp_f32 v95, v95\n"
"v_smfmac_f32_32x32x32_bf16 v[76:91], v[52:55], v[132:139], v9\n"
"s_add_u32 m0, s48, 65664\n"
"ds_read_b128 v[52:55], v16 offset:0\n"
"buffer_load_dwordx4 v10, s[20:23], s84 offen sc1 lds\n"
"ds_read_b64_tr_b16 v[150:151], v21 offset:1024\n"
"s_waitcnt lgkmcnt(4)\n"
"s_setprio 2\n"
"v_exp_f32 v96, v96\n"
"v_exp_f32 v97, v97\n"
"v_exp_f32 v98, v98\n"
"v_exp_f32 v99, v99\n"
"v_mfma_f32_32x32x16_bf16 v[108:123], v[44:47], v[36:39], v[108:123]\n"
"s_add_u32 m0, s48, 147616\n"
"s_mov_b32 s17, 172032\n"
"ds_read_b128 v[44:47], v12 offset:17440\n"
"buffer_load_dwordx4 v18, s[24:27], s17 offen lds\n"
"v_exp_f32 v100, v100\n"
"v_exp_f32 v101, v101\n"
"v_exp_f32 v102, v102\n"
"v_exp_f32 v103, v103\n"
"v_smfmac_f32_32x32x32_bf16 v[76:91], v[48:51], v[92:99], v9\n"
"ds_read_b128 v[48:51], v14 offset:8208\n"
"s_waitcnt lgkmcnt(3)\n"
"s_setprio 1\n"
"v_exp_f32 v104, v104\n"
"v_exp_f32 v105, v105\n"
"v_exp_f32 v106, v106\n"
"v_exp_f32 v107, v107\n"
"v_mfma_f32_32x32x16_bf16 v[124:139], v[40:43], v[32:35], v[60:75]\n"
"ds_read_b128 v[40:43], v12 offset:24624\n"
"ds_read_b64_tr_b16 v[152:153], v20 offset:40960\n"
"v_exp_f32 v108, v108\n"
"v_exp_f32 v109, v109\n"
"v_exp_f32 v110, v110\n"
"v_exp_f32 v111, v111\n"
"v_smfmac_f32_32x32x32_bf16 v[76:91], v[52:55], v[100:107], v9\n"
"s_add_u32 m0, s48, 73872\n"
"ds_read_b128 v[52:55], v16 offset:8208\n"
"buffer_load_dwordx4 v10, s[20:23], s85 offen sc1 lds\n"
"ds_read_b64_tr_b16 v[154:155], v20 offset:41984\n"
"s_waitcnt lgkmcnt(4)\n"
"s_setprio 0\n"
"v_exp_f32 v112, v112\n"
"v_exp_f32 v113, v113\n"
"v_exp_f32 v114, v114\n"
"v_exp_f32 v115, v115\n"
"v_mfma_f32_32x32x16_bf16 v[124:139], v[44:47], v[36:39], v[124:139]\n"
"s_add_u32 m0, s48, 139424\n"
"s_mov_b32 s17, 200704\n"
"ds_read_b128 v[44:47], v12 offset:25648\n"
"buffer_load_dwordx4 v18, s[24:27], s17 offen lds\n"
"v_exp_f32 v116, v116\n"
"v_exp_f32 v117, v117\n"
"v_exp_f32 v118, v118\n"
"v_exp_f32 v119, v119\n"
"v_smfmac_f32_32x32x32_bf16 v[76:91], v[48:51], v[108:115], v9\n"
"ds_read_b128 v[48:51], v14 offset:16416\n"
"s_waitcnt vmcnt(4) lgkmcnt(2)\n"
"s_barrier\n"
"s_setprio 3\n"
"v_exp_f32 v120, v120\n"
"v_exp_f32 v121, v121\n"
"v_exp_f32 v122, v122\n"
"v_exp_f32 v123, v123\n"
"v_mfma_f32_32x32x16_bf16 v[92:107], v[40:43], v[32:35], v[60:75]\n"
"ds_read_b128 v[40:43], v12 offset:32832\n"
"ds_read_b64_tr_b16 v[156:157], v20 offset:32768\n"
"v_exp_f32 v124, v124\n"
"v_exp_f32 v125, v125\n"
"v_exp_f32 v126, v126\n"
"v_exp_f32 v127, v127\n"
"v_smfmac_f32_32x32x32_bf16 v[76:91], v[52:55], v[116:123], v9\n"
"s_add_u32 m0, s48, 0\n"
"ds_read_b128 v[52:55], v16 offset:16416\n"
"buffer_load_dwordx4 v10, s[20:23], s86 offen sc1 lds\n"
"ds_read_b64_tr_b16 v[158:159], v20 offset:33792\n"
"s_waitcnt lgkmcnt(4)\n"
"s_setprio 2\n"
"v_exp_f32 v128, v128\n"
"v_exp_f32 v129, v129\n"
"v_exp_f32 v130, v130\n"
"v_exp_f32 v131, v131\n"
"v_mfma_f32_32x32x16_bf16 v[92:107], v[44:47], v[36:39], v[92:107]\n"
"ds_read_b128 v[44:47], v12 offset:33856\n"
"v_exp_f32 v132, v132\n"
"v_exp_f32 v133, v133\n"
"v_exp_f32 v134, v134\n"
"v_exp_f32 v135, v135\n"
"v_smfmac_f32_32x32x32_bf16 v[76:91], v[48:51], v[124:131], v9\n"
"ds_read_b128 v[48:51], v14 offset:24624\n"
"s_waitcnt lgkmcnt(3)\n"
"s_setprio 1\n"
"v_exp_f32 v136, v136\n"
"v_exp_f32 v137, v137\n"
"v_exp_f32 v138, v138\n"
"v_exp_f32 v139, v139\n"
"v_mfma_f32_32x32x16_bf16 v[108:123], v[40:43], v[32:35], v[60:75]\n"
"ds_read_b128 v[40:43], v12 offset:41040\n"
"ds_read_b64_tr_b16 v[160:161], v20 offset:24576\n"
"v_exp_f32 v92, v92\n"
"v_exp_f32 v93, v93\n"
"v_exp_f32 v94, v94\n"
"v_exp_f32 v95, v95\n"
"v_smfmac_f32_32x32x32_bf16 v[76:91], v[52:55], v[132:139], v9\n"
"s_add_u32 m0, s48, 8208\n"
"ds_read_b128 v[52:55], v16 offset:24624\n"
"buffer_load_dwordx4 v10, s[20:23], s87 offen sc1 lds\n"
"ds_read_b64_tr_b16 v[162:163], v20 offset:25600\n"
"s_waitcnt lgkmcnt(4)\n"
"s_setprio 0\n"
"v_exp_f32 v96, v96\n"
"v_exp_f32 v97, v97\n"
"v_exp_f32 v98, v98\n"
"v_exp_f32 v99, v99\n"
"v_mfma_f32_32x32x16_bf16 v[108:123], v[44:47], v[36:39], v[108:123]\n"
"ds_read_b128 v[44:47], v12 offset:42064\n"
"v_exp_f32 v100, v100\n"
"v_exp_f32 v101, v101\n"
"v_exp_f32 v102, v102\n"
"v_exp_f32 v103, v103\n"
"v_smfmac_f32_32x32x32_bf16 v[76:91], v[48:51], v[92:99], v9\n"
"ds_read_b128 v[48:51], v14 offset:32832\n"
"s_waitcnt vmcnt(2) lgkmcnt(2)\n"
"s_barrier\n"
"s_setprio 3\n"
"v_exp_f32 v104, v104\n"
"v_exp_f32 v105, v105\n"
"v_exp_f32 v106, v106\n"
"v_exp_f32 v107, v107\n"
"v_mfma_f32_32x32x16_bf16 v[124:139], v[40:43], v[32:35], v[60:75]\n"
"ds_read_b128 v[40:43], v12 offset:49248\n"
"ds_read_b64_tr_b16 v[164:165], v21 offset:16384\n"
"v_exp_f32 v108, v108\n"
"v_exp_f32 v109, v109\n"
"v_exp_f32 v110, v110\n"
"v_exp_f32 v111, v111\n"
"v_smfmac_f32_32x32x32_bf16 v[76:91], v[52:55], v[100:107], v9\n"
"s_add_u32 m0, s48, 16416\n"
"ds_read_b128 v[52:55], v16 offset:32832\n"
"buffer_load_dwordx4 v10, s[20:23], s88 offen sc1 lds\n"
"ds_read_b64_tr_b16 v[166:167], v21 offset:17408\n"
"s_waitcnt lgkmcnt(4)\n"
"s_setprio 2\n"
"v_exp_f32 v112, v112\n"
"v_exp_f32 v113, v113\n"
"v_exp_f32 v114, v114\n"
"v_exp_f32 v115, v115\n"
"v_mfma_f32_32x32x16_bf16 v[124:139], v[44:47], v[36:39], v[124:139]\n"
"ds_read_b128 v[44:47], v12 offset:50272\n"
"v_exp_f32 v116, v116\n"
"v_exp_f32 v117, v117\n"
"v_exp_f32 v118, v118\n"
"v_exp_f32 v119, v119\n"
"v_smfmac_f32_32x32x32_bf16 v[76:91], v[48:51], v[108:115], v9\n"
"ds_read_b128 v[48:51], v14 offset:41040\n"
"s_waitcnt lgkmcnt(3)\n"
"s_setprio 1\n"
"v_exp_f32 v120, v120\n"
"v_exp_f32 v121, v121\n"
"v_exp_f32 v122, v122\n"
"v_exp_f32 v123, v123\n"
"v_mfma_f32_32x32x16_bf16 v[92:107], v[40:43], v[32:35], v[60:75]\n"
"ds_read_b128 v[40:43], v13 offset:0\n"
"ds_read_b64_tr_b16 v[168:169], v21 offset:8192\n"
"v_exp_f32 v124, v124\n"
"v_exp_f32 v125, v125\n"
"v_exp_f32 v126, v126\n"
"v_exp_f32 v127, v127\n"
"v_smfmac_f32_32x32x32_bf16 v[76:91], v[52:55], v[116:123], v9\n"
"s_add_u32 m0, s48, 24624\n"
"ds_read_b128 v[52:55], v16 offset:41040\n"
"buffer_load_dwordx4 v10, s[20:23], s89 offen sc1 lds\n"
"ds_read_b64_tr_b16 v[170:171], v21 offset:9216\n"
"s_waitcnt lgkmcnt(4)\n"
"s_setprio 0\n"
"v_exp_f32 v128, v128\n"
"v_exp_f32 v129, v129\n"
"v_exp_f32 v130, v130\n"
"v_exp_f32 v131, v131\n"
"v_mfma_f32_32x32x16_bf16 v[92:107], v[44:47], v[36:39], v[92:107]\n"
"ds_read_b128 v[44:47], v13 offset:1024\n"
"v_exp_f32 v132, v132\n"
"v_exp_f32 v133, v133\n"
"v_exp_f32 v134, v134\n"
"v_exp_f32 v135, v135\n"
"v_smfmac_f32_32x32x32_bf16 v[76:91], v[48:51], v[124:131], v9\n"
"ds_read_b128 v[48:51], v14 offset:49248\n"
"s_waitcnt lgkmcnt(2)\n"
"s_barrier\n"
"s_setprio 3\n"
"v_exp_f32 v136, v136\n"
"v_exp_f32 v137, v137\n"
"v_exp_f32 v138, v138\n"
"v_exp_f32 v139, v139\n"
"v_mfma_f32_32x32x16_bf16 v[108:123], v[40:43], v[32:35], v[60:75]\n"
"ds_read_b128 v[40:43], v13 offset:8208\n"
"v_exp_f32 v92, v92\n"
"v_exp_f32 v93, v93\n"
"v_exp_f32 v94, v94\n"
"v_exp_f32 v95, v95\n"
"v_smfmac_f32_32x32x32_bf16 v[76:91], v[52:55], v[132:139], v9\n"
"s_add_u32 m0, s48, 32832\n"
"ds_read_b128 v[52:55], v16 offset:49248\n"
"buffer_load_dwordx4 v10, s[20:23], s90 offen sc1 lds\n"
"s_waitcnt lgkmcnt(2)\n"
"s_setprio 2\n"
"v_exp_f32 v96, v96\n"
"v_exp_f32 v97, v97\n"
"v_exp_f32 v98, v98\n"
"v_exp_f32 v99, v99\n"
"v_mfma_f32_32x32x16_bf16 v[108:123], v[44:47], v[36:39], v[108:123]\n"
"s_add_u32 m0, s48, 147616\n"
"s_mov_b32 s17, 229376\n"
"ds_read_b128 v[44:47], v13 offset:9232\n"
"buffer_load_dwordx4 v18, s[24:27], s17 offen lds\n"
"v_exp_f32 v100, v100\n"
"v_exp_f32 v101, v101\n"
"v_exp_f32 v102, v102\n"
"v_exp_f32 v103, v103\n"
"v_smfmac_f32_32x32x32_bf16 v[76:91], v[48:51], v[92:99], v9\n"
"ds_read_b128 v[48:51], v15 offset:0\n"
"s_waitcnt lgkmcnt(2)\n"
"s_setprio 1\n"
"v_exp_f32 v104, v104\n"
"v_exp_f32 v105, v105\n"
"v_exp_f32 v106, v106\n"
"v_exp_f32 v107, v107\n"
"v_mfma_f32_32x32x16_bf16 v[124:139], v[40:43], v[32:35], v[60:75]\n"
"ds_read_b128 v[40:43], v13 offset:16416\n"
"v_exp_f32 v108, v108\n"
"v_exp_f32 v109, v109\n"
"v_exp_f32 v110, v110\n"
"v_exp_f32 v111, v111\n"
"v_smfmac_f32_32x32x32_bf16 v[76:91], v[52:55], v[100:107], v9\n"
"s_add_u32 m0, s48, 41040\n"
"ds_read_b128 v[52:55], v17 offset:0\n"
"buffer_load_dwordx4 v10, s[20:23], s91 offen sc1 lds\n"
"s_waitcnt lgkmcnt(2)\n"
"s_setprio 0\n"
"v_exp_f32 v112, v112\n"
"v_exp_f32 v113, v113\n"
"v_exp_f32 v114, v114\n"
"v_exp_f32 v115, v115\n"
"v_mfma_f32_32x32x16_bf16 v[124:139], v[44:47], v[36:39], v[124:139]\n"
"s_add_u32 m0, s48, 139424\n"
"s_mov_b32 s17, 258048\n"
"ds_read_b128 v[44:47], v13 offset:17440\n"
"buffer_load_dwordx4 v18, s[24:27], s17 offen lds\n"
"v_exp_f32 v116, v116\n"
"v_exp_f32 v117, v117\n"
"v_exp_f32 v118, v118\n"
"v_exp_f32 v119, v119\n"
"v_smfmac_f32_32x32x32_bf16 v[76:91], v[48:51], v[108:115], v9\n"
"ds_read_b128 v[48:51], v15 offset:8208\n"
"s_waitcnt vmcnt(6) lgkmcnt(2)\n"
"s_barrier\n"
"s_setprio 3\n"
"v_exp_f32 v120, v120\n"
"v_exp_f32 v121, v121\n"
"v_exp_f32 v122, v122\n"
"v_exp_f32 v123, v123\n"
"v_mfma_f32_32x32x16_bf16 v[92:107], v[40:43], v[32:35], v[60:75]\n"
"ds_read_b128 v[40:43], v12 offset:0\n"
"v_exp_f32 v124, v124\n"
"v_exp_f32 v125, v125\n"
"v_exp_f32 v126, v126\n"
"v_exp_f32 v127, v127\n"
"v_smfmac_f32_32x32x32_bf16 v[76:91], v[52:55], v[116:123], v9\n"
"s_add_u32 m0, s48, 49248\n"
"ds_read_b128 v[52:55], v17 offset:8208\n"
"buffer_load_dwordx4 v10, s[20:23], s92 offen sc1 lds\n"
"s_waitcnt lgkmcnt(2)\n"
"s_setprio 2\n"
"v_exp_f32 v128, v128\n"
"v_exp_f32 v129, v129\n"
"v_exp_f32 v130, v130\n"
"v_exp_f32 v131, v131\n"
"v_mfma_f32_32x32x16_bf16 v[92:107], v[44:47], v[36:39], v[92:107]\n"
"s_add_u32 m0, s48, 98464\n"
"s_mov_b32 s17, 286720\n"
"ds_read_b128 v[44:47], v12 offset:1024\n"
"buffer_load_dwordx4 v18, s[24:27], s17 offen lds\n"
"v_exp_f32 v132, v132\n"
"v_exp_f32 v133, v133\n"
"v_exp_f32 v134, v134\n"
"v_exp_f32 v135, v135\n"
"v_smfmac_f32_32x32x32_bf16 v[76:91], v[48:51], v[124:131], v9\n"
"ds_read_b128 v[48:51], v15 offset:16416\n"
"s_waitcnt lgkmcnt(2)\n"
"s_setprio 1\n"
"v_exp_f32 v136, v136\n"
"v_exp_f32 v137, v137\n"
"v_exp_f32 v138, v138\n"
"v_exp_f32 v139, v139\n"
"v_mfma_f32_32x32x16_bf16 v[108:123], v[40:43], v[32:35], v[60:75]\n"
"ds_read_b128 v[40:43], v12 offset:8208\n"
"v_exp_f32 v92, v92\n"
"v_exp_f32 v93, v93\n"
"v_exp_f32 v94, v94\n"
"v_exp_f32 v95, v95\n"
"v_smfmac_f32_32x32x32_bf16 v[76:91], v[52:55], v[132:139], v9\n"
"s_add_u32 m0, s48, 57456\n"
"ds_read_b128 v[52:55], v17 offset:16416\n"
"buffer_load_dwordx4 v10, s[20:23], s93 offen sc1 lds\n"
"s_waitcnt lgkmcnt(2)\n"
"s_setprio 0\n"
"v_exp_f32 v96, v96\n"
"v_exp_f32 v97, v97\n"
"v_exp_f32 v98, v98\n"
"v_exp_f32 v99, v99\n"
"v_mfma_f32_32x32x16_bf16 v[108:123], v[44:47], v[36:39], v[108:123]\n"
"s_add_u32 m0, s48, 106656\n"
"s_mov_b32 s17, 315392\n"
"ds_read_b128 v[44:47], v12 offset:9232\n"
"buffer_load_dwordx4 v18, s[24:27], s17 offen lds\n"
"v_exp_f32 v100, v100\n"
"v_exp_f32 v101, v101\n"
"v_exp_f32 v102, v102\n"
"v_exp_f32 v103, v103\n"
"v_smfmac_f32_32x32x32_bf16 v[76:91], v[48:51], v[92:99], v9\n"
"ds_read_b128 v[48:51], v14 offset:0\n"
"s_waitcnt vmcnt(4) lgkmcnt(2)\n"
"s_barrier\n"
"s_setprio 3\n"
"v_exp_f32 v104, v104\n"
"v_exp_f32 v105, v105\n"
"v_exp_f32 v106, v106\n"
"v_exp_f32 v107, v107\n"
"v_mfma_f32_32x32x16_bf16 v[124:139], v[40:43], v[32:35], v[60:75]\n"
"ds_read_b128 v[40:43], v12 offset:16416\n"
"ds_read_b64_tr_b16 v[172:173], v21 offset:16384\n"
"v_exp_f32 v108, v108\n"
"v_exp_f32 v109, v109\n"
"v_exp_f32 v110, v110\n"
"v_exp_f32 v111, v111\n"
"v_smfmac_f32_32x32x32_bf16 v[76:91], v[52:55], v[100:107], v9\n"
"s_add_u32 m0, s48, 65664\n"
"ds_read_b128 v[52:55], v16 offset:0\n"
"buffer_load_dwordx4 v10, s[20:23], s94 offen sc1 lds\n"
"ds_read_b64_tr_b16 v[174:175], v21 offset:17408\n"
"s_waitcnt lgkmcnt(4)\n"
"s_setprio 2\n"
"v_exp_f32 v112, v112\n"
"v_exp_f32 v113, v113\n"
"v_exp_f32 v114, v114\n"
"v_exp_f32 v115, v115\n"
"v_mfma_f32_32x32x16_bf16 v[124:139], v[44:47], v[36:39], v[124:139]\n"
"s_add_u32 m0, s48, 82080\n"
"s_mov_b32 s17, 344064\n"
"ds_read_b128 v[44:47], v12 offset:17440\n"
"buffer_load_dwordx4 v18, s[24:27], s17 offen lds\n"
"v_exp_f32 v116, v116\n"
"v_exp_f32 v117, v117\n"
"v_exp_f32 v118, v118\n"
"v_exp_f32 v119, v119\n"
"v_smfmac_f32_32x32x32_bf16 v[76:91], v[48:51], v[108:115], v9\n"
"ds_read_b128 v[48:51], v14 offset:8208\n"
"s_waitcnt lgkmcnt(3)\n"
"s_setprio 1\n"
"v_exp_f32 v120, v120\n"
"v_exp_f32 v121, v121\n"
"v_exp_f32 v122, v122\n"
"v_exp_f32 v123, v123\n"
"v_mfma_f32_32x32x16_bf16 v[92:107], v[40:43], v[32:35], v[60:75]\n"
"ds_read_b128 v[40:43], v12 offset:24624\n"
"ds_read_b64_tr_b16 v[176:177], v21 offset:8192\n"
"v_exp_f32 v124, v124\n"
"v_exp_f32 v125, v125\n"
"v_exp_f32 v126, v126\n"
"v_exp_f32 v127, v127\n"
"v_smfmac_f32_32x32x32_bf16 v[76:91], v[52:55], v[116:123], v9\n"
"s_add_u32 m0, s48, 73872\n"
"ds_read_b128 v[52:55], v16 offset:8208\n"
"buffer_load_dwordx4 v10, s[20:23], s95 offen sc1 lds\n"
"ds_read_b64_tr_b16 v[178:179], v21 offset:9216\n"
"s_waitcnt lgkmcnt(4)\n"
"s_setprio 0\n"
"v_exp_f32 v128, v128\n"
"v_exp_f32 v129, v129\n"
"v_exp_f32 v130, v130\n"
"v_exp_f32 v131, v131\n"
"v_mfma_f32_32x32x16_bf16 v[92:107], v[44:47], v[36:39], v[92:107]\n"
"s_add_u32 m0, s48, 90272\n"
"s_mov_b32 s17, 372736\n"
"ds_read_b128 v[44:47], v12 offset:25648\n"
"buffer_load_dwordx4 v18, s[24:27], s17 offen lds\n"
"v_exp_f32 v132, v132\n"
"v_exp_f32 v133, v133\n"
"v_exp_f32 v134, v134\n"
"v_exp_f32 v135, v135\n"
"v_smfmac_f32_32x32x32_bf16 v[76:91], v[48:51], v[124:131], v9\n"
"ds_read_b128 v[48:51], v14 offset:16416\n"
"s_waitcnt vmcnt(4) lgkmcnt(2)\n"
"s_barrier\n"
"s_setprio 3\n"
"v_exp_f32 v136, v136\n"
"v_exp_f32 v137, v137\n"
"v_exp_f32 v138, v138\n"
"v_exp_f32 v139, v139\n"
"v_mfma_f32_32x32x16_bf16 v[108:123], v[40:43], v[32:35], v[60:75]\n"
"ds_read_b128 v[40:43], v12 offset:32832\n"
"ds_read_b64_tr_b16 v[180:181], v20 offset:16384\n"
"v_exp_f32 v92, v92\n"
"v_exp_f32 v93, v93\n"
"v_exp_f32 v94, v94\n"
"v_exp_f32 v95, v95\n"
"v_smfmac_f32_32x32x32_bf16 v[76:91], v[52:55], v[132:139], v9\n"
"s_add_u32 m0, s48, 147616\n"
"s_mov_b32 s17, 0\n"
"ds_read_b128 v[52:55], v16 offset:16416\n"
"buffer_load_dwordx4 v19, s[28:31], s17 offen lds\n"
"ds_read_b64_tr_b16 v[182:183], v20 offset:17408\n"
"s_waitcnt lgkmcnt(4)\n"
"s_setprio 2\n"
"v_exp_f32 v96, v96\n"
"v_exp_f32 v97, v97\n"
"v_exp_f32 v98, v98\n"
"v_exp_f32 v99, v99\n"
"v_mfma_f32_32x32x16_bf16 v[108:123], v[44:47], v[36:39], v[108:123]\n"
"s_add_u32 m0, s48, 139424\n"
"s_mov_b32 s17, 57344\n"
"ds_read_b128 v[44:47], v12 offset:33856\n"
"buffer_load_dwordx4 v19, s[28:31], s17 offen lds\n"
"v_exp_f32 v100, v100\n"
"v_exp_f32 v101, v101\n"
"v_exp_f32 v102, v102\n"
"v_exp_f32 v103, v103\n"
"v_smfmac_f32_32x32x32_bf16 v[76:91], v[48:51], v[92:99], v9\n"
"ds_read_b128 v[48:51], v14 offset:24624\n"
"s_waitcnt lgkmcnt(3)\n"
"s_setprio 1\n"
"v_exp_f32 v104, v104\n"
"v_exp_f32 v105, v105\n"
"v_exp_f32 v106, v106\n"
"v_exp_f32 v107, v107\n"
"v_mfma_f32_32x32x16_bf16 v[124:139], v[40:43], v[32:35], v[60:75]\n"
"ds_read_b128 v[40:43], v12 offset:41040\n"
"ds_read_b64_tr_b16 v[184:185], v20 offset:24576\n"
"v_exp_f32 v108, v108\n"
"v_exp_f32 v109, v109\n"
"v_exp_f32 v110, v110\n"
"v_exp_f32 v111, v111\n"
"v_smfmac_f32_32x32x32_bf16 v[76:91], v[52:55], v[100:107], v9\n"
"s_add_u32 m0, s48, 131232\n"
"s_mov_b32 s17, 114688\n"
"ds_read_b128 v[52:55], v16 offset:24624\n"
"buffer_load_dwordx4 v19, s[28:31], s17 offen lds\n"
"ds_read_b64_tr_b16 v[186:187], v20 offset:25600\n"
"s_waitcnt lgkmcnt(4)\n"
"s_setprio 0\n"
"v_exp_f32 v112, v112\n"
"v_exp_f32 v113, v113\n"
"v_exp_f32 v114, v114\n"
"v_exp_f32 v115, v115\n"
"v_mfma_f32_32x32x16_bf16 v[124:139], v[44:47], v[36:39], v[124:139]\n"
"s_add_u32 m0, s48, 123040\n"
"s_mov_b32 s17, 172032\n"
"ds_read_b128 v[44:47], v12 offset:42064\n"
"buffer_load_dwordx4 v19, s[28:31], s17 offen lds\n"
"v_exp_f32 v116, v116\n"
"v_exp_f32 v117, v117\n"
"v_exp_f32 v118, v118\n"
"v_exp_f32 v119, v119\n"
"v_smfmac_f32_32x32x32_bf16 v[76:91], v[48:51], v[108:115], v9\n"
"ds_read_b128 v[48:51], v14 offset:32832\n"
"s_waitcnt vmcnt(4) lgkmcnt(2)\n"
"s_barrier\n"
"s_setprio 3\n"
"v_exp_f32 v120, v120\n"
"v_exp_f32 v121, v121\n"
"v_exp_f32 v122, v122\n"
"v_exp_f32 v123, v123\n"
"v_mfma_f32_32x32x16_bf16 v[92:107], v[40:43], v[32:35], v[60:75]\n"
"ds_read_b128 v[40:43], v12 offset:49248\n"
"ds_read_b64_tr_b16 v[188:189], v20 offset:0\n"
"v_exp_f32 v124, v124\n"
"v_exp_f32 v125, v125\n"
"v_exp_f32 v126, v126\n"
"v_exp_f32 v127, v127\n"
"v_smfmac_f32_32x32x32_bf16 v[76:91], v[52:55], v[116:123], v9\n"
"s_add_u32 m0, s48, 114848\n"
"s_mov_b32 s17, 229376\n"
"ds_read_b128 v[52:55], v16 offset:32832\n"
"buffer_load_dwordx4 v19, s[28:31], s17 offen lds\n"
"ds_read_b64_tr_b16 v[190:191], v20 offset:1024\n"
"s_waitcnt lgkmcnt(4)\n"
"s_setprio 2\n"
"v_exp_f32 v128, v128\n"
"v_exp_f32 v129, v129\n"
"v_exp_f32 v130, v130\n"
"v_exp_f32 v131, v131\n"
"v_mfma_f32_32x32x16_bf16 v[92:107], v[44:47], v[36:39], v[92:107]\n"
"s_add_u32 m0, s48, 106656\n"
"s_mov_b32 s17, 286720\n"
"ds_read_b128 v[44:47], v12 offset:50272\n"
"buffer_load_dwordx4 v19, s[28:31], s17 offen lds\n"
"v_exp_f32 v132, v132\n"
"v_exp_f32 v133, v133\n"
"v_exp_f32 v134, v134\n"
"v_exp_f32 v135, v135\n"
"v_smfmac_f32_32x32x32_bf16 v[76:91], v[48:51], v[124:131], v9\n"
"ds_read_b128 v[48:51], v14 offset:41040\n"
"s_waitcnt lgkmcnt(3)\n"
"s_setprio 1\n"
"v_exp_f32 v136, v136\n"
"v_exp_f32 v137, v137\n"
"v_exp_f32 v138, v138\n"
"v_exp_f32 v139, v139\n"
"v_mfma_f32_32x32x16_bf16 v[108:123], v[40:43], v[32:35], v[60:75]\n"
"ds_read_b128 v[40:43], v13 offset:0\n"
"ds_read_b64_tr_b16 v[192:193], v20 offset:8192\n"
"v_exp_f32 v92, v92\n"
"v_exp_f32 v93, v93\n"
"v_exp_f32 v94, v94\n"
"v_exp_f32 v95, v95\n"
"v_smfmac_f32_32x32x32_bf16 v[76:91], v[52:55], v[132:139], v9\n"
"s_add_u32 m0, s48, 98464\n"
"s_mov_b32 s17, 344064\n"
"ds_read_b128 v[52:55], v16 offset:41040\n"
"buffer_load_dwordx4 v19, s[28:31], s17 offen lds\n"
"ds_read_b64_tr_b16 v[194:195], v20 offset:9216\n"
"s_waitcnt lgkmcnt(4)\n"
"s_setprio 0\n"
"v_exp_f32 v96, v96\n"
"v_exp_f32 v97, v97\n"
"v_exp_f32 v98, v98\n"
"v_exp_f32 v99, v99\n"
"v_mfma_f32_32x32x16_bf16 v[108:123], v[44:47], v[36:39], v[108:123]\n"
"s_add_u32 s16, s10, s48\n"
"s_add_u32 m0, s48, 0\n"
"s_add_u32 s17, s16, 0\n"
"ds_read_b128 v[44:47], v13 offset:1024\n"
"buffer_load_dwordx4 v4, s[20:23], s17 offen sc1 lds\n"
"v_exp_f32 v100, v100\n"
"v_exp_f32 v101, v101\n"
"v_exp_f32 v102, v102\n"
"v_exp_f32 v103, v103\n"
"v_smfmac_f32_32x32x32_bf16 v[76:91], v[48:51], v[92:99], v9\n"
"ds_read_b128 v[48:51], v14 offset:49248\n"
"s_waitcnt lgkmcnt(2)\n"
"s_barrier\n"
"s_setprio 3\n"
"v_exp_f32 v104, v104\n"
"v_exp_f32 v105, v105\n"
"v_exp_f32 v106, v106\n"
"v_exp_f32 v107, v107\n"
"v_mfma_f32_32x32x16_bf16 v[124:139], v[40:43], v[32:35], v[60:75]\n"
"ds_read_b128 v[40:43], v13 offset:8208\n"
"v_exp_f32 v108, v108\n"
"v_exp_f32 v109, v109\n"
"v_exp_f32 v110, v110\n"
"v_exp_f32 v111, v111\n"
"v_smfmac_f32_32x32x32_bf16 v[76:91], v[52:55], v[100:107], v9\n"
"s_add_u32 m0, s48, 8192\n"
"s_add_u32 s17, s16, 8192\n"
"ds_read_b128 v[52:55], v16 offset:49248\n"
"buffer_load_dwordx4 v4, s[20:23], s17 offen sc1 lds\n"
"s_waitcnt lgkmcnt(2)\n"
"s_setprio 2\n"
"v_exp_f32 v112, v112\n"
"v_exp_f32 v113, v113\n"
"v_exp_f32 v114, v114\n"
"v_exp_f32 v115, v115\n"
"v_mfma_f32_32x32x16_bf16 v[124:139], v[44:47], v[36:39], v[124:139]\n"
"s_add_u32 m0, s48, 16384\n"
"s_add_u32 s17, s16, 16384\n"
"ds_read_b128 v[44:47], v13 offset:9232\n"
"buffer_load_dwordx4 v4, s[20:23], s17 offen sc1 lds\n"
"v_exp_f32 v116, v116\n"
"v_exp_f32 v117, v117\n"
"v_exp_f32 v118, v118\n"
"v_exp_f32 v119, v119\n"
"v_smfmac_f32_32x32x32_bf16 v[76:91], v[48:51], v[108:115], v9\n"
"ds_read_b128 v[48:51], v15 offset:0\n"
"s_waitcnt lgkmcnt(2)\n"
"s_setprio 1\n"
"v_exp_f32 v120, v120\n"
"v_exp_f32 v121, v121\n"
"v_exp_f32 v122, v122\n"
"v_exp_f32 v123, v123\n"
"v_mfma_f32_32x32x16_bf16 v[92:107], v[40:43], v[32:35], v[60:75]\n"
"ds_read_b128 v[40:43], v13 offset:16416\n"
"v_exp_f32 v124, v124\n"
"v_exp_f32 v125, v125\n"
"v_exp_f32 v126, v126\n"
"v_exp_f32 v127, v127\n"
"v_smfmac_f32_32x32x32_bf16 v[76:91], v[52:55], v[116:123], v9\n"
"s_add_u32 m0, s48, 24576\n"
"s_add_u32 s17, s16, 24576\n"
"ds_read_b128 v[52:55], v17 offset:0\n"
"buffer_load_dwordx4 v4, s[20:23], s17 offen sc1 lds\n"
"s_waitcnt lgkmcnt(2)\n"
"s_setprio 0\n"
"v_exp_f32 v128, v128\n"
"v_exp_f32 v129, v129\n"
"v_exp_f32 v130, v130\n"
"v_exp_f32 v131, v131\n"
"v_mfma_f32_32x32x16_bf16 v[92:107], v[44:47], v[36:39], v[92:107]\n"
"s_add_u32 m0, s48, 32768\n"
"s_add_u32 s17, s16, 32768\n"
"ds_read_b128 v[44:47], v13 offset:17440\n"
"buffer_load_dwordx4 v4, s[20:23], s17 offen sc1 lds\n"
"v_exp_f32 v132, v132\n"
"v_exp_f32 v133, v133\n"
"v_exp_f32 v134, v134\n"
"v_exp_f32 v135, v135\n"
"v_smfmac_f32_32x32x32_bf16 v[76:91], v[48:51], v[124:131], v9\n"
"ds_read_b128 v[48:51], v15 offset:8208\n"
"s_waitcnt lgkmcnt(2)\n"
"s_barrier\n"
"s_setprio 3\n"
"v_exp_f32 v136, v136\n"
"v_exp_f32 v137, v137\n"
"v_exp_f32 v138, v138\n"
"v_exp_f32 v139, v139\n"
"v_mfma_f32_32x32x16_bf16 v[108:123], v[40:43], v[32:35], v[60:75]\n"
"v_exp_f32 v92, v92\n"
"v_exp_f32 v93, v93\n"
"v_exp_f32 v94, v94\n"
"v_exp_f32 v95, v95\n"
"v_smfmac_f32_32x32x32_bf16 v[76:91], v[52:55], v[132:139], v9\n"
"s_add_u32 m0, s48, 40960\n"
"s_add_u32 s17, s16, 40960\n"
"ds_read_b128 v[52:55], v17 offset:8208\n"
"buffer_load_dwordx4 v4, s[20:23], s17 offen sc1 lds\n"
"s_waitcnt lgkmcnt(1)\n"
"s_setprio 2\n"
"v_exp_f32 v96, v96\n"
"v_exp_f32 v97, v97\n"
"v_exp_f32 v98, v98\n"
"v_exp_f32 v99, v99\n"
"v_mfma_f32_32x32x16_bf16 v[108:123], v[44:47], v[36:39], v[108:123]\n"
"s_add_u32 m0, s48, 49152\n"
"s_add_u32 s17, s16, 49152\n"
"buffer_load_dwordx4 v4, s[20:23], s17 offen sc1 lds\n"
"v_exp_f32 v100, v100\n"
"v_exp_f32 v101, v101\n"
"v_exp_f32 v102, v102\n"
"v_exp_f32 v103, v103\n"
"v_smfmac_f32_32x32x32_bf16 v[76:91], v[48:51], v[92:99], v9\n"
"ds_read_b128 v[48:51], v15 offset:16416\n"
"s_waitcnt lgkmcnt(1)\n"
"s_setprio 1\n"
"v_exp_f32 v104, v104\n"
"v_exp_f32 v105, v105\n"
"v_exp_f32 v106, v106\n"
"v_exp_f32 v107, v107\n"
"s_nop 0\n"
"v_exp_f32 v108, v108\n"
"v_exp_f32 v109, v109\n"
"v_exp_f32 v110, v110\n"
"v_exp_f32 v111, v111\n"
"v_smfmac_f32_32x32x32_bf16 v[76:91], v[52:55], v[100:107], v9\n"
"ds_read_b128 v[52:55], v17 offset:16416\n"
"s_waitcnt lgkmcnt(1)\n"
"s_setprio 0\n"
"v_exp_f32 v112, v112\n"
"v_exp_f32 v113, v113\n"
"v_exp_f32 v114, v114\n"
"v_exp_f32 v115, v115\n"
"v_exp_f32 v116, v116\n"
"v_exp_f32 v117, v117\n"
"v_exp_f32 v118, v118\n"
"v_exp_f32 v119, v119\n"
"v_smfmac_f32_32x32x32_bf16 v[76:91], v[48:51], v[108:115], v9\n"
"s_waitcnt lgkmcnt(0)\n"
"s_setprio 3\n"
"v_exp_f32 v120, v120\n"
"v_exp_f32 v121, v121\n"
"v_exp_f32 v122, v122\n"
"v_exp_f32 v123, v123\n"
"s_nop 1\n"
"v_smfmac_f32_32x32x32_bf16 v[76:91], v[52:55], v[116:123], v9\n"
"s_setprio 0\n"
"s_add_u32 s14, s49, 14680064\n"
"s_cmp_eq_u32 s43, 0\n"
"s_cselect_b32 s15, 2130706432, s14\n"
"s_cmp_eq_u32 s43, 0\n"
"s_cselect_b64 vcc, -1, 0\n"
"s_nop 5\n"
"ds_write_b128 v28, v[84:87]\n"
"v_cndmask_b32 v5, v76, v88, vcc\n"
"ds_write_b128 v28, v[88:91] offset:1024\n"
"ds_write_b32 v30, v5\n"
"s_waitcnt vmcnt(0) lgkmcnt(0)\n"
"s_barrier\n"
"ds_read2st64_b32 v[24:25], v31 offset0:0 offset1:1\n"
"ds_read_b128 v[92:95], v29\n"
"ds_read_b128 v[96:99], v29 offset:1024\n"
"s_waitcnt lgkmcnt(2)\n"
"v_add_f32 v7, v24, v25\n"
"v_cmp_nlt_f32 vcc, 0x7800000, v7\n"
"v_rcp_f32 v6, v7\n"
"s_waitcnt lgkmcnt(0)\n"
"v_add_f32 v92, v92, v76\n"
"v_add_f32 v93, v93, v77\n"
"v_add_f32 v94, v94, v78\n"
"v_add_f32 v95, v95, v79\n"
"v_add_f32 v96, v96, v80\n"
"v_add_f32 v97, v97, v81\n"
"v_add_f32 v98, v98, v82\n"
"v_add_f32 v99, v99, v83\n"
"v_add_f32 v8, abs(v92), v7\n"
"v_add_f32 v5, abs(v93), abs(v94)\n"
"v_add_f32 v8, abs(v95), v8\n"
"v_add_f32 v5, abs(v96), v5\n"
"v_add_f32 v8, abs(v97), v8\n"
"v_add_f32 v5, abs(v98), v5\n"
"v_add_f32 v8, abs(v99), v8\n"
"s_mov_b64 s[52:53], vcc\n"
"v_add_f32 v8, v5, v8\n"
"v_cmp_ngt_f32 vcc, 0x7b800000, v8\n"
"s_or_b64 vcc, vcc, s[52:53]\n"
"s_cbranch_vccnz .Lfused_fwd_slow\n"
".Lfused_fwd_slow_r0:\n"
"v_mul_f32 v92, v92, v6\n"
"v_mul_f32 v93, v93, v6\n"
"v_mul_f32 v94, v94, v6\n"
"v_mul_f32 v95, v95, v6\n"
"v_mul_f32 v96, v96, v6\n"
"v_mul_f32 v97, v97, v6\n"
"v_mul_f32 v98, v98, v6\n"
"v_mul_f32 v99, v99, v6\n"
"v_cvt_pk_bf16_f32 v100, v92, v93\n"
"v_cvt_pk_bf16_f32 v101, v94, v95\n"
"v_cvt_pk_bf16_f32 v102, v96, v97\n"
"v_cvt_pk_bf16_f32 v103, v98, v99\n"
"v_min_u32 v100, -2, v100\n"
"v_min_u32 v102, -2, v102\n"
"v_min_u32 v101, -2, v101\n"
"v_min_u32 v103, -2, v103\n"
"v_permlane32_swap_b32 v100, v102\n"
"s_nop 0\n"
"v_permlane32_swap_b32 v101, v103\n"
"buffer_store_dwordx4 v[100:103], v56, s[20:23], s14 offen sc1\n"
"buffer_store_dwordx2 v[102:103], v11, s[20:23], s15 offen sc1\n"
"s_add_u32 s14, s11, s48\n"
"v_add_u32 v5, s48, v4\n"
"v_add_u32 v6, 155808, v5\n"
"v_add_u32 v5, 57456, v5\n"
"s_add_u32 s64, s14, 0\n"
"s_add_u32 s65, s14, 4096\n"
"s_add_u32 s66, s14, 8192\n"
"s_add_u32 s67, s14, 12288\n"
"s_add_u32 s68, s14, 16384\n"
"s_add_u32 s69, s14, 20480\n"
"s_add_u32 s70, s14, 24576\n"
"s_mov_b32 s51, 0\n"
"s_waitcnt vmcnt(0)\n"
"buffer_load_dwordx4 v[108:111], v4, s[20:23], s64 offen sc1\n"
"buffer_load_dwordx4 v[112:115], v4, s[20:23], s65 offen sc1\n"
"buffer_load_dwordx4 v[116:119], v4, s[20:23], s66 offen sc1\n"
"buffer_load_dwordx4 v[120:123], v4, s[20:23], s67 offen sc1\n"
"buffer_load_dwordx4 v[124:127], v4, s[20:23], s68 offen sc1\n"
"buffer_load_dwordx4 v[128:131], v4, s[20:23], s69 offen sc1\n"
"buffer_load_dwordx4 v[132:135], v4, s[20:23], s70 offen sc1\n"
"s_waitcnt vmcnt(6)\n"
"ds_write_b128 v5, v[108:111] offset:0\n"
"v_max3_u32 v7, v108, v109, v110\n"
"v_max_u32 v7, v7, v111\n"
"v_cmp_eq_u32 vcc, -1, v7\n"
"s_cmp_lg_u64 vcc, 0\n"
"s_cselect_b32 s52, 1, 0\n"
"s_or_b32 s51, s51, s52\n"
"s_waitcnt vmcnt(5)\n"
"ds_write_b128 v5, v[112:115] offset:4096\n"
"v_max3_u32 v7, v112, v113, v114\n"
"v_max_u32 v7, v7, v115\n"
"v_cmp_eq_u32 vcc, -1, v7\n"
"s_cmp_lg_u64 vcc, 0\n"
"s_cselect_b32 s52, 2, 0\n"
"s_or_b32 s51, s51, s52\n"
"s_waitcnt vmcnt(4)\n"
"ds_write_b128 v5, v[116:119] offset:8192\n"
"v_max3_u32 v7, v116, v117, v118\n"
"v_max_u32 v7, v7, v119\n"
"v_cmp_eq_u32 vcc, -1, v7\n"
"s_cmp_lg_u64 vcc, 0\n"
"s_cselect_b32 s52, 4, 0\n"
"s_or_b32 s51, s51, s52\n"
"s_waitcnt vmcnt(3)\n"
"ds_write_b128 v5, v[120:123] offset:12288\n"
"v_max3_u32 v7, v120, v121, v122\n"
"v_max_u32 v7, v7, v123\n"
"v_cmp_eq_u32 vcc, -1, v7\n"
"s_cmp_lg_u64 vcc, 0\n"
"s_cselect_b32 s52, 8, 0\n"
"s_or_b32 s51, s51, s52\n"
"s_waitcnt vmcnt(2)\n"
"ds_write_b128 v5, v[124:127] offset:16384\n"
"v_max3_u32 v7, v124, v125, v126\n"
"v_max_u32 v7, v7, v127\n"
"v_cmp_eq_u32 vcc, -1, v7\n"
"s_cmp_lg_u64 vcc, 0\n"
"s_cselect_b32 s52, 16, 0\n"
"s_or_b32 s51, s51, s52\n"
"s_waitcnt vmcnt(1)\n"
"ds_write_b128 v5, v[128:131] offset:20480\n"
"v_max3_u32 v7, v128, v129, v130\n"
"v_max_u32 v7, v7, v131\n"
"v_cmp_eq_u32 vcc, -1, v7\n"
"s_cmp_lg_u64 vcc, 0\n"
"s_cselect_b32 s52, 32, 0\n"
"s_or_b32 s51, s51, s52\n"
"s_waitcnt vmcnt(0)\n"
"ds_write_b128 v6, v[132:135]\n"
"v_max3_u32 v7, v132, v133, v134\n"
"v_max_u32 v7, v7, v135\n"
"v_cmp_eq_u32 vcc, -1, v7\n"
"s_cmp_lg_u64 vcc, 0\n"
"s_cselect_b32 s52, 64, 0\n"
"s_or_b32 s51, s51, s52\n"
"s_waitcnt lgkmcnt(0)\n"
"s_cbranch_scc1 .Lfused_fwd_obretry\n"
".Lfused_fwd_final_c:\n"
"v_lshrrev_b32 v5, 4, v1\n"
"v_and_b32 v6, 15, v1\n"
"v_lshlrev_b32 v6, 4, v6\n"
"v_lshl_add_u32 v5, v5, 9, v6\n"
"v_add_u32 v6, 155808, v5\n"
"v_add_u32 v5, 57456, v5\n"
"s_barrier\n"
"ds_read_b128 v[76:79], v5 offset:0\n"
"ds_read_b128 v[80:83], v5 offset:256\n"
"ds_read_b128 v[84:87], v5 offset:2048\n"
"ds_read_b128 v[88:91], v5 offset:2304\n"
"ds_read_b128 v[92:95], v5 offset:4096\n"
"ds_read_b128 v[96:99], v5 offset:4352\n"
"ds_read_b128 v[100:103], v5 offset:6144\n"
"ds_read_b128 v[104:107], v5 offset:6400\n"
"ds_read_b128 v[108:111], v5 offset:8192\n"
"ds_read_b128 v[112:115], v5 offset:8448\n"
"ds_read_b128 v[116:119], v5 offset:10240\n"
"ds_read_b128 v[120:123], v5 offset:10496\n"
"s_waitcnt lgkmcnt(10)\n"
"v_mfma_f32_16x16x32_bf16 v[32:35], v[140:143], v[76:79], 0\n"
"v_mfma_f32_16x16x32_bf16 v[36:39], v[140:143], v[80:83], 0\n"
"ds_read_b128 v[124:127], v5 offset:12288\n"
"ds_read_b128 v[128:131], v5 offset:12544\n"
"s_waitcnt lgkmcnt(10)\n"
"v_mfma_f32_16x16x32_bf16 v[32:35], v[144:147], v[84:87], v[32:35]\n"
"v_mfma_f32_16x16x32_bf16 v[36:39], v[144:147], v[88:91], v[36:39]\n"
"ds_read_b128 v[76:79], v5 offset:14336\n"
"ds_read_b128 v[80:83], v5 offset:14592\n"
"s_waitcnt lgkmcnt(10)\n"
"v_mfma_f32_16x16x32_bf16 v[32:35], v[148:151], v[92:95], v[32:35]\n"
"v_mfma_f32_16x16x32_bf16 v[36:39], v[148:151], v[96:99], v[36:39]\n"
"ds_read_b128 v[84:87], v5 offset:16384\n"
"ds_read_b128 v[88:91], v5 offset:16640\n"
"s_waitcnt lgkmcnt(10)\n"
"v_mfma_f32_16x16x32_bf16 v[32:35], v[152:155], v[100:103], v[32:35]\n"
"v_mfma_f32_16x16x32_bf16 v[36:39], v[152:155], v[104:107], v[36:39]\n"
"ds_read_b128 v[92:95], v5 offset:18432\n"
"ds_read_b128 v[96:99], v5 offset:18688\n"
"s_waitcnt lgkmcnt(10)\n"
"v_mfma_f32_16x16x32_bf16 v[32:35], v[156:159], v[108:111], v[32:35]\n"
"v_mfma_f32_16x16x32_bf16 v[36:39], v[156:159], v[112:115], v[36:39]\n"
"ds_read_b128 v[100:103], v5 offset:20480\n"
"ds_read_b128 v[104:107], v5 offset:20736\n"
"s_waitcnt lgkmcnt(10)\n"
"v_mfma_f32_16x16x32_bf16 v[32:35], v[160:163], v[116:119], v[32:35]\n"
"v_mfma_f32_16x16x32_bf16 v[36:39], v[160:163], v[120:123], v[36:39]\n"
"ds_read_b128 v[108:111], v5 offset:22528\n"
"ds_read_b128 v[112:115], v5 offset:22784\n"
"s_waitcnt lgkmcnt(10)\n"
"v_mfma_f32_16x16x32_bf16 v[32:35], v[164:167], v[124:127], v[32:35]\n"
"v_mfma_f32_16x16x32_bf16 v[36:39], v[164:167], v[128:131], v[36:39]\n"
"ds_read_b128 v[116:119], v6 offset:0\n"
"ds_read_b128 v[120:123], v6 offset:256\n"
"s_waitcnt lgkmcnt(10)\n"
"v_mfma_f32_16x16x32_bf16 v[32:35], v[168:171], v[76:79], v[32:35]\n"
"v_mfma_f32_16x16x32_bf16 v[36:39], v[168:171], v[80:83], v[36:39]\n"
"ds_read_b128 v[124:127], v6 offset:2048\n"
"ds_read_b128 v[128:131], v6 offset:2304\n"
"s_waitcnt lgkmcnt(10)\n"
"v_mfma_f32_16x16x32_bf16 v[32:35], v[172:175], v[84:87], v[32:35]\n"
"v_mfma_f32_16x16x32_bf16 v[36:39], v[172:175], v[88:91], v[36:39]\n"
"s_waitcnt lgkmcnt(8)\n"
"v_mfma_f32_16x16x32_bf16 v[32:35], v[176:179], v[92:95], v[32:35]\n"
"v_mfma_f32_16x16x32_bf16 v[36:39], v[176:179], v[96:99], v[36:39]\n"
"s_waitcnt lgkmcnt(6)\n"
"v_mfma_f32_16x16x32_bf16 v[32:35], v[180:183], v[100:103], v[32:35]\n"
"v_mfma_f32_16x16x32_bf16 v[36:39], v[180:183], v[104:107], v[36:39]\n"
"s_waitcnt lgkmcnt(4)\n"
"v_mfma_f32_16x16x32_bf16 v[32:35], v[184:187], v[108:111], v[32:35]\n"
"v_mfma_f32_16x16x32_bf16 v[36:39], v[184:187], v[112:115], v[36:39]\n"
"s_waitcnt lgkmcnt(2)\n"
"v_mfma_f32_16x16x32_bf16 v[32:35], v[188:191], v[116:119], v[32:35]\n"
"v_mfma_f32_16x16x32_bf16 v[36:39], v[188:191], v[120:123], v[36:39]\n"
"s_waitcnt lgkmcnt(0)\n"
"v_mfma_f32_16x16x32_bf16 v[32:35], v[192:195], v[124:127], v[32:35]\n"
"v_mfma_f32_16x16x32_bf16 v[36:39], v[192:195], v[128:131], v[36:39]\n"
"s_nop 6\n"
"v_cvt_pk_bf16_f32 v40, v32, v33\n"
"v_cvt_pk_bf16_f32 v42, v36, v37\n"
"v_cvt_pk_bf16_f32 v41, v34, v35\n"
"v_cvt_pk_bf16_f32 v43, v38, v39\n"
"v_permlane16_swap_b32 v40, v42\n"
"s_nop 0\n"
"v_permlane16_swap_b32 v41, v43\n"
"buffer_store_dwordx4 v[40:43], v58, s[32:35], 0 offen\n"
"s_endpgm\n"
".Lfused_fwd_spin_g:\n"
"s_mov_b32 s63, 200000\n"
".Lfused_fwd_spin_g_1:\n"
"s_nop 0\n"
"buffer_load_dwordx2 v[26:27], v22, s[20:23], 0 offen sc1\n"
"s_waitcnt vmcnt(0)\n"
"v_cmp_ne_u64 vcc, s[36:37], v[26:27]\n"
"s_cbranch_vccz .Lfused_fwd_spin_g_r\n"
"s_sub_u32 s63, s63, 1\n"
"s_cmp_lg_u32 s63, 0\n"
"s_cbranch_scc1 .Lfused_fwd_spin_g_1\n"
"s_branch .Lfused_fwd_spin_g_r\n"
".Lfused_fwd_spin_i:\n"
"s_mov_b32 s63, 200000\n"
".Lfused_fwd_spin_i_1:\n"
"s_sleep 3\n"
"buffer_load_dwordx2 v[26:27], v22, s[20:23], 0 offen sc1\n"
"s_waitcnt vmcnt(0)\n"
"v_cmp_ne_u64 vcc, s[36:37], v[26:27]\n"
"s_cbranch_vccz .Lfused_fwd_spin_i_r\n"
"s_sub_u32 s63, s63, 1\n"
"s_cmp_lg_u32 s63, 0\n"
"s_cbranch_scc1 .Lfused_fwd_spin_i_1\n"
"s_branch .Lfused_fwd_spin_i_r\n"
".Lfused_fwd_spin_a:\n"
"s_mov_b32 s63, 200000\n"
".Lfused_fwd_spin_a_1:\n"
"s_nop 0\n"
"buffer_load_dwordx2 v[26:27], v23, s[20:23], 0 offen sc1\n"
"s_waitcnt vmcnt(0)\n"
"v_cmp_ne_u64 vcc, s[36:37], v[26:27]\n"
"s_cbranch_vccz .Lfused_fwd_spin_a_r\n"
"s_sub_u32 s63, s63, 1\n"
"s_cmp_lg_u32 s63, 0\n"
"s_cbranch_scc1 .Lfused_fwd_spin_a_1\n"
"s_branch .Lfused_fwd_spin_a_r\n"
".Lfused_fwd_aretry:\n"
"s_add_u32 s63, s63, 8\n"
"s_cmp_lt_u32 s63, 200000\n"
"s_cbranch_scc0 .Lfused_fwd_aretry_r\n"
"s_add_u32 s16, s15, s10\n"
"s_mov_b32 s50, s15\n"
"s_mov_b32 s51, 28\n"
".Lfused_fwd_aretry1:\n"
"s_mov_b32 m0, s50\n"
"s_add_u32 s50, s50, 1024\n"
"buffer_load_dwordx4 v4, s[20:23], s16 offen sc1 lds\n"
"s_add_u32 s16, s16, 1024\n"
"s_sub_u32 s51, s51, 1\n"
"s_cmp_lg_u32 s51, 0\n"
"s_cbranch_scc1 .Lfused_fwd_aretry1\n"
"s_waitcnt vmcnt(0)\n"
"s_branch .Lfused_fwd_aproj\n"
".Lfused_fwd_obretry:\n"
"s_mov_b32 s50, 200000\n"
".Lfused_fwd_obretry1:\n"
"s_mov_b32 s53, s51\n"
"s_mov_b32 s51, 0\n"
"s_bitcmp1_b32 s53, 0\n"
"s_cbranch_scc0 .Lfused_fwd_obs0\n"
"buffer_load_dwordx4 v[108:111], v4, s[20:23], s64 offen sc1\n"
".Lfused_fwd_obs0:\n"
"s_bitcmp1_b32 s53, 1\n"
"s_cbranch_scc0 .Lfused_fwd_obs1\n"
"buffer_load_dwordx4 v[112:115], v4, s[20:23], s65 offen sc1\n"
".Lfused_fwd_obs1:\n"
"s_bitcmp1_b32 s53, 2\n"
"s_cbranch_scc0 .Lfused_fwd_obs2\n"
"buffer_load_dwordx4 v[116:119], v4, s[20:23], s66 offen sc1\n"
".Lfused_fwd_obs2:\n"
"s_bitcmp1_b32 s53, 3\n"
"s_cbranch_scc0 .Lfused_fwd_obs3\n"
"buffer_load_dwordx4 v[120:123], v4, s[20:23], s67 offen sc1\n"
".Lfused_fwd_obs3:\n"
"s_bitcmp1_b32 s53, 4\n"
"s_cbranch_scc0 .Lfused_fwd_obs4\n"
"buffer_load_dwordx4 v[124:127], v4, s[20:23], s68 offen sc1\n"
".Lfused_fwd_obs4:\n"
"s_bitcmp1_b32 s53, 5\n"
"s_cbranch_scc0 .Lfused_fwd_obs5\n"
"buffer_load_dwordx4 v[128:131], v4, s[20:23], s69 offen sc1\n"
".Lfused_fwd_obs5:\n"
"s_bitcmp1_b32 s53, 6\n"
"s_cbranch_scc0 .Lfused_fwd_obs6\n"
"buffer_load_dwordx4 v[132:135], v4, s[20:23], s70 offen sc1\n"
".Lfused_fwd_obs6:\n"
"s_waitcnt vmcnt(0)\n"
"s_bitcmp1_b32 s53, 0\n"
"s_cbranch_scc0 .Lfused_fwd_obt0\n"
"ds_write_b128 v5, v[108:111] offset:0\n"
"v_max3_u32 v7, v108, v109, v110\n"
"v_max_u32 v7, v7, v111\n"
"v_cmp_eq_u32 vcc, -1, v7\n"
"s_cmp_lg_u64 vcc, 0\n"
"s_cselect_b32 s52, 1, 0\n"
"s_or_b32 s51, s51, s52\n"
".Lfused_fwd_obt0:\n"
"s_bitcmp1_b32 s53, 1\n"
"s_cbranch_scc0 .Lfused_fwd_obt1\n"
"ds_write_b128 v5, v[112:115] offset:4096\n"
"v_max3_u32 v7, v112, v113, v114\n"
"v_max_u32 v7, v7, v115\n"
"v_cmp_eq_u32 vcc, -1, v7\n"
"s_cmp_lg_u64 vcc, 0\n"
"s_cselect_b32 s52, 2, 0\n"
"s_or_b32 s51, s51, s52\n"
".Lfused_fwd_obt1:\n"
"s_bitcmp1_b32 s53, 2\n"
"s_cbranch_scc0 .Lfused_fwd_obt2\n"
"ds_write_b128 v5, v[116:119] offset:8192\n"
"v_max3_u32 v7, v116, v117, v118\n"
"v_max_u32 v7, v7, v119\n"
"v_cmp_eq_u32 vcc, -1, v7\n"
"s_cmp_lg_u64 vcc, 0\n"
"s_cselect_b32 s52, 4, 0\n"
"s_or_b32 s51, s51, s52\n"
".Lfused_fwd_obt2:\n"
"s_bitcmp1_b32 s53, 3\n"
"s_cbranch_scc0 .Lfused_fwd_obt3\n"
"ds_write_b128 v5, v[120:123] offset:12288\n"
"v_max3_u32 v7, v120, v121, v122\n"
"v_max_u32 v7, v7, v123\n"
"v_cmp_eq_u32 vcc, -1, v7\n"
"s_cmp_lg_u64 vcc, 0\n"
"s_cselect_b32 s52, 8, 0\n"
"s_or_b32 s51, s51, s52\n"
".Lfused_fwd_obt3:\n"
"s_bitcmp1_b32 s53, 4\n"
"s_cbranch_scc0 .Lfused_fwd_obt4\n"
"ds_write_b128 v5, v[124:127] offset:16384\n"
"v_max3_u32 v7, v124, v125, v126\n"
"v_max_u32 v7, v7, v127\n"
"v_cmp_eq_u32 vcc, -1, v7\n"
"s_cmp_lg_u64 vcc, 0\n"
"s_cselect_b32 s52, 16, 0\n"
"s_or_b32 s51, s51, s52\n"
".Lfused_fwd_obt4:\n"
"s_bitcmp1_b32 s53, 5\n"
"s_cbranch_scc0 .Lfused_fwd_obt5\n"
"ds_write_b128 v5, v[128:131] offset:20480\n"
"v_max3_u32 v7, v128, v129, v130\n"
"v_max_u32 v7, v7, v131\n"
"v_cmp_eq_u32 vcc, -1, v7\n"
"s_cmp_lg_u64 vcc, 0\n"
"s_cselect_b32 s52, 32, 0\n"
"s_or_b32 s51, s51, s52\n"
".Lfused_fwd_obt5:\n"
"s_bitcmp1_b32 s53, 6\n"
"s_cbranch_scc0 .Lfused_fwd_obt6\n"
"ds_write_b128 v6, v[132:135]\n"
"v_max3_u32 v7, v132, v133, v134\n"
"v_max_u32 v7, v7, v135\n"
"v_cmp_eq_u32 vcc, -1, v7\n"
"s_cmp_lg_u64 vcc, 0\n"
"s_cselect_b32 s52, 64, 0\n"
"s_or_b32 s51, s51, s52\n"
".Lfused_fwd_obt6:\n"
"s_waitcnt lgkmcnt(0)\n"
"s_cmp_eq_u32 s51, 0\n"
"s_cbranch_scc1 .Lfused_fwd_final_c\n"
"s_sub_u32 s50, s50, 1\n"
"s_cmp_lg_u32 s50, 0\n"
"s_cbranch_scc1 .Lfused_fwd_obretry1\n"
"s_branch .Lfused_fwd_final_c\n"
".Lfused_fwd_slow:\n"
"v_mov_b32 v76, 0\n"
"v_mov_b32 v77, 0\n"
"v_mov_b32 v78, 0\n"
"v_mov_b32 v79, 0\n"
"v_mov_b32 v80, 0\n"
"v_mov_b32 v81, 0\n"
"v_mov_b32 v82, 0\n"
"v_mov_b32 v83, 0\n"
"v_mov_b32 v84, 0\n"
"v_mov_b32 v85, 0\n"
"v_mov_b32 v86, 0\n"
"v_mov_b32 v87, 0\n"
"v_mov_b32 v88, 0\n"
"v_mov_b32 v89, 0\n"
"v_mov_b32 v90, 0\n"
"v_mov_b32 v91, 0\n"
"v_mov_b32 v48, 0x3f803f80\n"
"v_mov_b32 v49, 0x3f803f80\n"
"v_mov_b32 v50, 0x3f803f80\n"
"v_mov_b32 v51, 0x3f803f80\n"
"v_mov_b32 v52, 0x3f803f80\n"
"v_mov_b32 v53, 0x3f803f80\n"
"v_mov_b32 v54, 0x3f803f80\n"
"v_mov_b32 v55, 0x3f803f80\n"
"v_mov_b32 v24, 0xff61b1e6\n"
"s_add_u32 s50, s47, 2097152\n"
"s_mov_b32 s51, 4194304\n"
"s_mov_b32 s53, 64\n"
".Lfused_fwd_slow1:\n"
"buffer_load_dwordx4 v[40:43], v4, s[20:23], s50 offen sc1\n"
"buffer_load_dwordx4 v[44:47], v4, s[20:23], s50 offen offset:1024 sc1\n"
"s_add_u32 s52, s51, 14336\n"
"s_mov_b64 exec, s[8:9]\n"
"buffer_load_dwordx4 v[48:51], v57, s[20:23], s51 offen sc1\n"
"buffer_load_dwordx4 v[52:55], v57, s[20:23], s52 offen sc1\n"
"s_mov_b64 exec, -1\n"
"s_waitcnt vmcnt(0)\n"
"v_mfma_f32_32x32x16_bf16 v[108:123], v[40:43], v[32:35], 0\n"
"v_mfma_f32_32x32x16_bf16 v[108:123], v[44:47], v[36:39], v[108:123]\n"
"s_nop 11\n"
"v_max3_f32 v7, v108, v109, v110\n"
"v_max3_f32 v8, v111, v112, v113\n"
"v_max3_f32 v7, v7, v114, v115\n"
"v_max3_f32 v8, v8, v116, v117\n"
"v_max3_f32 v7, v7, v118, v119\n"
"v_max3_f32 v8, v8, v120, v121\n"
"v_max3_f32 v7, v7, v122, v123\n"
"v_max_f32 v7, v7, v8\n"
"v_mov_b32 v8, v7\n"
"s_nop 1\n"
"v_permlane32_swap_b32 v7, v8\n"
"v_max_f32 v7, v7, v8\n"
"v_max_f32 v25, v24, v7\n"
"v_sub_f32 v7, v24, v25\n"
"v_exp_f32 v7, v7\n"
"v_mov_b32 v24, v25\n"
"v_mul_f32 v76, v76, v7\n"
"v_mul_f32 v77, v77, v7\n"
"v_mul_f32 v78, v78, v7\n"
"v_mul_f32 v79, v79, v7\n"
"v_mul_f32 v80, v80, v7\n"
"v_mul_f32 v81, v81, v7\n"
"v_mul_f32 v82, v82, v7\n"
"v_mul_f32 v83, v83, v7\n"
"v_mul_f32 v84, v84, v7\n"
"v_mul_f32 v85, v85, v7\n"
"v_mul_f32 v86, v86, v7\n"
"v_mul_f32 v87, v87, v7\n"
"v_mul_f32 v88, v88, v7\n"
"v_mul_f32 v89, v89, v7\n"
"v_mul_f32 v90, v90, v7\n"
"v_mul_f32 v91, v91, v7\n"
"v_sub_f32 v108, v108, v25\n"
"v_sub_f32 v109, v109, v25\n"
"v_sub_f32 v110, v110, v25\n"
"v_sub_f32 v111, v111, v25\n"
"v_sub_f32 v112, v112, v25\n"
"v_sub_f32 v113, v113, v25\n"
"v_sub_f32 v114, v114, v25\n"
"v_sub_f32 v115, v115, v25\n"
"v_sub_f32 v116, v116, v25\n"
"v_sub_f32 v117, v117, v25\n"
"v_sub_f32 v118, v118, v25\n"
"v_sub_f32 v119, v119, v25\n"
"v_sub_f32 v120, v120, v25\n"
"v_sub_f32 v121, v121, v25\n"
"v_sub_f32 v122, v122, v25\n"
"v_sub_f32 v123, v123, v25\n"
"v_exp_f32 v108, v108\n"
"v_exp_f32 v109, v109\n"
"v_exp_f32 v110, v110\n"
"v_exp_f32 v111, v111\n"
"v_exp_f32 v112, v112\n"
"v_exp_f32 v113, v113\n"
"v_exp_f32 v114, v114\n"
"v_exp_f32 v115, v115\n"
"v_exp_f32 v116, v116\n"
"v_exp_f32 v117, v117\n"
"v_exp_f32 v118, v118\n"
"v_exp_f32 v119, v119\n"
"v_exp_f32 v120, v120\n"
"v_exp_f32 v121, v121\n"
"v_exp_f32 v122, v122\n"
"v_exp_f32 v123, v123\n"
"s_add_u32 s50, s50, 28672\n"
"s_add_u32 s51, s51, 28672\n"
"v_smfmac_f32_32x32x32_bf16 v[76:91], v[48:51], v[108:115], v9\n"
"v_smfmac_f32_32x32x32_bf16 v[76:91], v[52:55], v[116:123], v9\n"
"s_sub_u32 s53, s53, 1\n"
"s_cmp_lg_u32 s53, 0\n"
"s_nop 11\n"
"s_cbranch_scc1 .Lfused_fwd_slow1\n"
"s_cmp_eq_u32 s43, 0\n"
"s_cselect_b64 vcc, -1, 0\n"
"v_cndmask_b32 v7, v76, v88, vcc\n"
"v_mov_b32 v8, v7\n"
"s_nop 1\n"
"v_permlane32_swap_b32 v7, v8\n"
"v_cndmask_b32 v7, v7, v8, vcc\n"
"v_rcp_f32 v6, v7\n"
"s_cmp_eq_u32 s45, 0\n"
"s_cbranch_scc0 .Lfused_fwd_slow2\n"
"v_mov_b32 v92, v76\n"
"v_mov_b32 v93, v77\n"
"v_mov_b32 v94, v78\n"
"v_mov_b32 v95, v79\n"
"v_mov_b32 v96, v80\n"
"v_mov_b32 v97, v81\n"
"v_mov_b32 v98, v82\n"
"v_mov_b32 v99, v83\n"
"s_branch .Lfused_fwd_slow_r0\n"
".Lfused_fwd_slow2:\n"
"v_mov_b32 v92, v84\n"
"v_mov_b32 v93, v85\n"
"v_mov_b32 v94, v86\n"
"v_mov_b32 v95, v87\n"
"v_mov_b32 v96, v88\n"
"v_mov_b32 v97, v89\n"
"v_mov_b32 v98, v90\n"
"v_mov_b32 v99, v91\n"
"s_branch .Lfused_fwd_slow_r1\n"
".Lfused_fwd_end:\n"
    : "+{s[0:1]}"(ka), "+{s2}"(bid), "+{v0}"(tid)
    : "s"((unsigned)(size_t)(__attribute__((address_space(3))) char*)lds)
    : "v1","v2","v3","v4","v5","v6","v7","v8","v9","v10","v11","v12","v13","v14","v15","v16","v17","v18","v19","v20","v21","v22","v23","v24","v25","v26","v27","v28","v29","v30","v31","v32","v33","v34","v35","v36","v37","v38","v39","v40","v41","v42","v43","v44","v45","v46","v47","v48","v49","v50","v51","v52","v53","v54","v55","v56","v57","v58","v59","v60","v61","v62","v63","v64","v65","v66","v67","v68","v69","v70","v71","v72","v73","v74","v75","v76","v77","v78","v79","v80","v81","v82","v83","v84","v85","v86","v87","v88","v89","v90","v91","v92","v93","v94","v95","v96","v97","v98","v99","v100","v101","v102","v103","v104","v105","v106","v107","v108","v109","v110","v111","v112","v113","v114","v115","v116","v117","v118","v119","v120","v121","v122","v123","v124","v125","v126","v127","v128","v129","v130","v131","v132","v133","v134","v135","v136","v137","v138","v139","v140","v141","v142","v143","v144","v145","v146","v147","v148","v149","v150","v151","v152","v153","v154","v155","v156","v157","v158","v159","v160","v161","v162","v163","v164","v165","v166","v167","v168","v169","v170","v171","v172","v173","v174","v175","v176","v177","v178","v179","v180","v181","v182","v183","v184","v185","v186","v187","v188","v189","v190","v191","v192","v193","v194","v195","v196","v197","v198","v199","v200","v201","v202","v203","v204","v205","v206","v207","v208","v209","v210","v211","v212","v213","v214","v215","v216","v217","v218","v219","v220","v221","v222","v223","v224","v225","v226","v227","v228","v229","v230","v231","v232","v233","v234","v235","v236","v237","v238","v239","v240","v241","v242","v243","v244","v245","v246","v247","v248","v249","v250","v251","v252","v253","v254","v255","s4","s5","s6","s7","s8","s9","s10","s11","s12","s13","s14","s15","s16","s17","s18","s19","s20","s21","s22","s23","s24","s25","s26","s27","s28","s29","s30","s31","s32","s33","s34","s35","s36","s37","s38","s39","s40","s41","s42","s43","s44","s45","s46","s47","s48","s49","s50","s51","s52","s53","s54","s55","s56","s57","s58","s59","s60","s61","s62","s63","s64","s65","s66","s67","s68","s69","s70","s71","s72","s73","s74","s75","s76","s77","s78","s79","s80","s81","s82","s83","s84","s85","s86","s87","s88","s89","s90","s91","s92","s93","s94","s95","vcc","scc","memory");
}

static void launch_fused(void* const* d_in, void* d_out, void* d_ws, hipStream_t stream) {
    const unsigned F = 0x20000u, NX = 2048u * 448u * 2u, NW = 448u * 1344u * 2u, NO = 448u * 448u * 2u;
    fused_fwd<<<256, 512, 0, stream>>>(d_in[3], NX, F, d_in[4], NW, F, d_in[0], NX, F, d_in[1], NW, F,
                                    d_ws, 18874368u, F, d_in[5], NO, F, d_in[2], NO, F, d_out, 2048u * 896u * 2u, F);
}

extern "C" void kernel_launch(void* const* d_in, const int* in_sizes, int n_in,
                              void* d_out, int out_size, void* d_ws, size_t ws_size,
                              hipStream_t stream) {
    launch_fused(d_in, d_out, d_ws, stream);
}
```
